```python
import torch

def _softmax(x, axis=-1):
    return torch.softmax(x, dim=axis)
def _gelu(x):
    return torch.nn.functional.gelu(x, approximate="tanh")
def _silu(x):
    return torch.nn.functional.silu(x)
def _sigmoid(x):
    return torch.sigmoid(x)
def _layernorm(x, axis=-1, eps=1e-5):
    m = x.mean(dim=axis, keepdim=True)
    v = x.var(dim=axis, keepdim=True, unbiased=False)
    return (x - m) / torch.sqrt(v + eps)
def _scatter_add(tgt, idx, src, axis=0):
    return tgt.clone().index_add_(axis, idx, src)
def _segment_sum(src, seg, n):
    out = torch.zeros((n,) + src.shape[1:], dtype=src.dtype)
    return out.index_add_(0, seg, src)
def _pad(x, pw):
    flat = tuple(v for pair in reversed(pw) for v in pair)
    return torch.nn.functional.pad(x, flat)

kernel_name = 'gated_mix__softmax_full_mha__dense_activation_mish__prompt'

def setup_inputs():

    _w = (lambda a: (lambda t: t.to(torch.bfloat16) if t.is_floating_point() else t)(torch.as_tensor(a)))
    def _A_setup():
        B, S, D = 1, 2048, 448
        return {
            "x": _w((lambda *s: torch.randn(*s, dtype=torch.bfloat16) * 0.05)(B, S, D)),
            "wqkv": _w((lambda *s: torch.randn(*s, dtype=torch.bfloat16) * 0.8)(D, 3 * D)),
            "wo": _w((lambda *s: torch.randn(*s, dtype=torch.bfloat16) * 0.05)(D, D)),
        }
    _A_in = _A_setup()

    def _B_setup():
        B, S, D, F = 1, 2048, 448, 1792
        return {
            "x": _w((lambda *s: torch.randn(*s, dtype=torch.bfloat16) * 0.05)(B, S, D)),
            "w1": _w((lambda *s: torch.randn(*s, dtype=torch.bfloat16) * 12.8)(D, F) * (D ** -0.5)),
            "b1": _w((lambda *s: torch.randn(*s, dtype=torch.bfloat16) * 0.1)(F)),
            "w2": _w((lambda *s: torch.randn(*s, dtype=torch.bfloat16) * 0.1)(F, D) * (F ** -0.5)),
        }
    _B_in = _B_setup()

    return {
        "A_x": _A_in["x"],
        "A_wqkv": _A_in["wqkv"],
        "A_wo": _A_in["wo"],
        "B_x": _B_in["x"],
        "B_w1": _B_in["w1"],
        "B_b1": _B_in["b1"],
        "B_w2": _B_in["w2"],
        "gate_w": _w((lambda *s: torch.randn(*s, dtype=torch.bfloat16) * 0.05)(448) * 16.0),
    }

def reference(A_x, A_wqkv, A_wo, B_x, B_w1, B_b1, B_w2, gate_w):
    def _A_ref(x, wqkv, wo):
        B, S, D = x.shape
        H = 16; Dh = D // H
        qkv = (lambda x, *d: x.permute(*d))((x @ wqkv).reshape(B, S, 3, H, Dh), 2, 0, 3, 1, 4)
        q, k, v = qkv[0], qkv[1], qkv[2]
        scale = Dh ** -0.5
        scores = torch.einsum("bhqd,bhkd->bhqk", q, k) * scale
        p = _softmax(scores, axis=-1)
        o = torch.einsum("bhqk,bhkd->bhqd", p, v)
        return (lambda x, *d: x.permute(*d))(o, 0, 2, 1, 3).reshape(B, S, D) @ wo

    def _B_ref(x, w1, b1, w2):
        h = torch.einsum("bsd,df->bsf", x, w1) + b1
        sp = torch.log1p(torch.exp(-torch.abs(h))) + torch.where(h > 0, h, torch.zeros_like(h))
        act = h * torch.tanh(sp)
        return torch.einsum("bsf,fd->bsd", act, w2)

    x = A_x
    g = _sigmoid((x * gate_w).sum(-1, keepdims=True))
    a = _A_ref(x, A_wqkv, A_wo)
    b = _B_ref(x, B_w1, B_b1, B_w2)
    return g * a + (1 - g) * b
```

```cpp
#include <hip/hip_runtime.h>
#include <stdint.h>
typedef __attribute__((ext_vector_type(8))) short bf16x8;
typedef __attribute__((ext_vector_type(4))) short s16x4;
typedef __attribute__((ext_vector_type(16))) float f32x16;
typedef __attribute__((ext_vector_type(4))) unsigned u32x4;
typedef __attribute__((ext_vector_type(2))) unsigned u32x2;
#define LAS __attribute__((address_space(3)))
#define MFMA(a, b, c) __builtin_amdgcn_mfma_f32_32x32x16_bf16(a, b, c, 0, 0, 0)
typedef __attribute__((ext_vector_type(8))) __bf16 bfv8;
static __device__ __forceinline__ f32x16 mfma(bf16x8 a, bf16x8 b, f32x16 c) { return __builtin_amdgcn_mfma_f32_32x32x16_bf16(__builtin_bit_cast(bfv8, a), __builtin_bit_cast(bfv8, b), c, 0, 0, 0); }
static __device__ __forceinline__ unsigned cvtpk(float lo, float hi) { unsigned r; asm("v_cvt_pk_bf16_f32 %0, %1, %2" : "=v"(r) : "v"(lo), "v"(hi)); return r; }
static __device__ __forceinline__ s16x4 trd(const LAS char* p) { return __builtin_amdgcn_ds_read_tr16_b64_v4i16((LAS s16x4*)p); }
static __device__ __forceinline__ bf16x8 cat(s16x4 a, s16x4 b) { return __builtin_shufflevector(a, b, 0, 1, 2, 3, 4, 5, 6, 7); }
static __device__ __forceinline__ float bf2f(unsigned short v) { return __uint_as_float((unsigned)v << 16); }
static __device__ __forceinline__ bf16x8 pack8(const f32x16& a, int s) {
  u32x4 w = {cvtpk(a[8*s], a[8*s+1]), cvtpk(a[8*s+2], a[8*s+3]), cvtpk(a[8*s+4], a[8*s+5]), cvtpk(a[8*s+6], a[8*s+7])};
  return __builtin_bit_cast(bf16x8, w);
}
constexpr int S = 2048, D = 448, F = 1792;
constexpr size_t KV_OFF = 0, Q_OFF = 4u << 20, ACT_OFF = 6u << 20, O_OFF = 14u << 20, G_OFF = 16u << 20;

__global__ __launch_bounds__(256) void k1(const unsigned short* x, const unsigned short* wqkv, const unsigned short* w1, const unsigned short* b1, const unsigned short* gw, char* ws) {
  __shared__ __attribute__((aligned(16))) char lds[448 * 72];
  const int h = blockIdx.x & 15, rb = blockIdx.x >> 4, w = threadIdx.x >> 6, l = threadIdx.x & 63, r = l & 31, hh = l >> 5;
  const int row = rb * 128 + w * 32 + r;
  bf16x8 xf[28];
#pragma unroll
  for (int s = 0; s < 28; ++s) xf[s] = *(const bf16x8*)(x + (size_t)row * D + 16 * s + 8 * hh);
  float gl = 0;
#pragma unroll
  for (int s = 0; s < 28; ++s) { bf16x8 g = *(const bf16x8*)(gw + 16 * s + 8 * hh);
#pragma unroll
    for (int j = 0; j < 8; ++j) gl += bf2f(xf[s][j]) * bf2f(g[j]); }
  gl += __shfl_xor(gl, 32);
  const float g = 1.f / (1.f + __expf(-gl));
  if (h == 0 && hh == 0) ((float*)(ws + G_OFF))[row] = g;
  const int nt = h < 14 ? 7 : 3;
  for (int t = 0; t < nt; ++t) {
    const unsigned short* src; int pitch, ncol;
    if (t < 3) { src = wqkv + t * 448 + h * 28; pitch = 1344; ncol = 28; } else { src = w1 + h * 128 + (t - 3) * 32; pitch = F; ncol = 32; }
    __syncthreads();
    for (int i = threadIdx.x; i < 448 * 8; i += 256) { int k = i >> 3, q = i & 7; u32x2 v = {0, 0}; if (q * 4 < ncol) v = *(const u32x2*)(src + (size_t)k * pitch + q * 4); *(u32x2*)(lds + k * 72 + q * 8) = v; }
    __syncthreads();
    const LAS char* base = (const LAS char*)lds + (8 * hh + ((l & 15) >> 2)) * 72 + ((l >> 4) & 1) * 32 + (l & 3) * 8;
    f32x16 acc = {};
    if (t == 2) {
#pragma unroll
      for (int s = 0; s < 28; ++s) acc = mfma(xf[s], cat(trd(base + s * 16 * 72), trd(base + (s * 16 + 4) * 72)), acc);
    } else {
#pragma unroll
      for (int s = 0; s < 28; ++s) acc = mfma(cat(trd(base + s * 16 * 72), trd(base + (s * 16 + 4) * 72)), xf[s], acc);
    }
    const int tile = rb * 4 + w;
    char* dst;
    if (t == 0) { const float sc = 0.1889822365046136f * 1.4426950408889634f;
#pragma unroll
      for (int i = 0; i < 16; ++i) acc[i] *= sc;
      dst = ws + Q_OFF + ((size_t)h * 64 + tile) * 2048;
    } else if (t == 1) { dst = ws + KV_OFF + ((size_t)h * 64 + tile) * 4096;
    } else if (t == 2) { if (r == 28) {
#pragma unroll
        for (int i = 0; i < 16; ++i) acc[i] = 1.f; }
      dst = ws + KV_OFF + ((size_t)h * 64 + tile) * 4096 + 2048;
    } else {
      const int f0 = h * 128 + (t - 3) * 32; const float c1 = 1.f - g, c2 = -2.f * c1;
#pragma unroll
      for (int i = 0; i < 16; ++i) { float hv = acc[i] + bf2f(b1[f0 + (i & 3) + 8 * (i >> 2) + 4 * hh]);
        float e = __builtin_amdgcn_exp2f(hv * 1.4426950408889634f), u = e + 1.f, den = __builtin_fmaf(u, u, 1.f), rr = __builtin_amdgcn_rcpf(den);
        acc[i] = hv * __builtin_fmaf(rr, c2, c1); }
      dst = ws + ACT_OFF + ((size_t)tile * 112 + f0 / 16) * 1024;
    }
    *(bf16x8*)(dst + l * 16) = pack8(acc, 0);
    *(bf16x8*)(dst + 1024 + l * 16) = pack8(acc, 1);
  }
}
__global__ __launch_bounds__(256) void k2(char* ws) {
  const int xcd = blockIdx.x & 7, i = blockIdx.x >> 3, h = 2 * xcd + (i & 1), rb = i >> 1, w = threadIdx.x >> 6, l = threadIdx.x & 63, r = l & 31, hh = l >> 5;
  const int tile = rb * 4 + w;
  const char* qp = ws + Q_OFF + ((size_t)h * 64 + tile) * 2048 + l * 16;
  const bf16x8 q0 = *(const bf16x8*)qp, q1 = *(const bf16x8*)(qp + 1024);
  const char* kv = ws + KV_OFF + (size_t)h * 64 * 4096 + l * 16;
  f32x16 o = {}; float m = -1e30f;
  for (int t = 0; t < 64; ++t) {
    const bf16x8 k0 = *(const bf16x8*)(kv + t * 4096), k1 = *(const bf16x8*)(kv + t * 4096 + 1024), v0 = *(const bf16x8*)(kv + t * 4096 + 2048), v1 = *(const bf16x8*)(kv + t * 4096 + 3072);
    f32x16 s = {}; s = mfma(k0, q0, s); s = mfma(k1, q1, s);
    float mx = s[0];
#pragma unroll
    for (int j = 1; j < 16; ++j) mx = fmaxf(mx, s[j]);
    mx = fmaxf(mx, __shfl_xor(mx, 32));
    if (__any(mx > m)) { float mn = fmaxf(m, mx), a = __builtin_amdgcn_exp2f(m - mn); m = mn;
#pragma unroll
      for (int j = 0; j < 16; ++j) o[j] *= a; }
#pragma unroll
    for (int j = 0; j < 16; ++j) s[j] = __builtin_amdgcn_exp2f(s[j] - m);
    o = mfma(v0, pack8(s, 0), o); o = mfma(v1, pack8(s, 1), o);
  }
  const float lsum = __shfl(o[12], r + 32);
  const float f = ((const float*)(ws + G_OFF))[tile * 32 + r] / lsum;
#pragma unroll
  for (int j = 0; j < 16; ++j) o[j] = (hh && j >= 12) ? 0.f : o[j] * f;
  char* dst = ws + O_OFF + ((size_t)tile * 32 + 2 * h) * 1024 + l * 16;
  *(bf16x8*)dst = pack8(o, 0); *(bf16x8*)(dst + 1024) = pack8(o, 1);
}
__global__ __launch_bounds__(256) void k3(const unsigned short* w2, const unsigned short* wo, const char* ws, unsigned short* out) {
  __shared__ __attribute__((aligned(16))) char lds[4 * 16384];
  const int xcd = blockIdx.x & 7, i = blockIdx.x >> 3, rt = xcd * 4 + i / 7, cg = i % 7, v = threadIdx.x >> 6, l = threadIdx.x & 63, r = l & 31, hh = l >> 5;
  char* my = lds + v * 16384;
  f32x16 acc[2][2] = {};
  const LAS char* base0 = (const LAS char*)my + (4 * hh + ((l & 15) >> 2)) * 128 + (l & 3) * 8 + ((l >> 4) & 1) * 32;
  const int sw = (((l & 15) >> 3) & 1) * 64;
  for (int c = 0; c < 5; ++c) {
    const unsigned short* src = c < 4 ? w2 + (size_t)(448 * v + 112 * c) * D : wo + (size_t)(112 * v) * D;
#pragma unroll
    for (int j = 0; j < 15; ++j) { int k = j * 8 + (l >> 3), q = l & 7; u32x4 d = {0, 0, 0, 0}; if (k < 112) d = *(const u32x4*)(src + (size_t)k * D + cg * 64 + q * 8);
      *(u32x4*)(my + k * 128 + ((q * 16) ^ (((k >> 1) & 1) * 64))) = d; }
    __builtin_amdgcn_s_waitcnt(0xC07F);
    const int ns = c < 4 ? 7 : 8;
    for (int s = 0; s < ns; ++s) {
      const int row0 = c < 4 ? 16 * s : 28 * (s >> 1) + 16 * (s & 1);
      const char* ap = c < 4 ? ws + ACT_OFF + ((size_t)(2 * rt) * 112 + 28 * v + 7 * c + s) * 1024 + l * 16 : ws + O_OFF + ((size_t)(2 * rt) * 32 + 8 * v + s) * 1024 + l * 16;
      const bf16x8 a0 = *(const bf16x8*)ap, a1 = *(const bf16x8*)(ap + (c < 4 ? 112 : 32) * 1024);
      const LAS char* b = base0 + row0 * 128;
      const bf16x8 w0 = cat(trd(b + sw), trd(b + 1024 + sw)), w1f = cat(trd(b + (64 ^ sw)), trd(b + 1024 + (64 ^ sw)));
      acc[0][0] = mfma(w0, a0, acc[0][0]); acc[0][1] = mfma(w1f, a0, acc[0][1]); acc[1][0] = mfma(w0, a1, acc[1][0]); acc[1][1] = mfma(w1f, a1, acc[1][1]);
    }
    __builtin_amdgcn_s_waitcnt(0xC07F);
  }
#pragma unroll
  for (int t = 0; t < 4; ++t)
#pragma unroll
    for (int j = 0; j < 16; ++j) *(float*)(my + t * 4096 + j * 256 + l * 4) = acc[t >> 1][t & 1][j];
  __syncthreads();
  float o[16];
#pragma unroll
  for (int j = 0; j < 16; ++j) { o[j] = 0;
#pragma unroll
    for (int u = 0; u < 4; ++u) o[j] += *(const float*)(lds + u * 16384 + v * 4096 + j * 256 + l * 4); }
  unsigned short* dst = out + (size_t)(rt * 64 + (v >> 1) * 32 + r) * D + cg * 64 + (v & 1) * 32 + 4 * hh;
#pragma unroll
  for (int gq = 0; gq < 4; ++gq) { u32x2 d = {cvtpk(o[4 * gq], o[4 * gq + 1]), cvtpk(o[4 * gq + 2], o[4 * gq + 3])}; *(u32x2*)(dst + 8 * gq) = d; }
}
extern "C" void kernel_launch(void* const* d_in, const int* in_sizes, int n_in, void* d_out, int out_size, void* d_ws, size_t ws_size, hipStream_t stream) {
  const unsigned short* x = (const unsigned short*)d_in[0]; const unsigned short* wqkv = (const unsigned short*)d_in[1]; const unsigned short* wo = (const unsigned short*)d_in[2];
  const unsigned short* w1 = (const unsigned short*)d_in[4]; const unsigned short* b1 = (const unsigned short*)d_in[5]; const unsigned short* w2 = (const unsigned short*)d_in[6]; const unsigned short* gw = (const unsigned short*)d_in[7];
  k1<<<256, 256, 0, stream>>>(x, wqkv, w1, b1, gw, (char*)d_ws);
  k2<<<256, 256, 0, stream>>>((char*)d_ws);
  k3<<<224, 256, 0, stream>>>(w2, wo, (const char*)d_ws, (unsigned short*)d_out);
}
```

```cpp
#include <hip/hip_runtime.h>

extern "C" __global__ __launch_bounds__(512, 2) void gated_mix_fused(
    const void* x, unsigned nx, unsigned f0, const void* wqkv, unsigned nwqkv, unsigned f1,
    const void* gw, unsigned ngw, unsigned f2, void* ws, unsigned nws, unsigned f3,
    const void* w1, unsigned nw1, unsigned f4, const void* b1, unsigned nb1, unsigned f5,
    const void* w2, unsigned nw2, unsigned f6, const void* wo, unsigned nwo, unsigned f7,
    void* out, unsigned nout, unsigned f8) {
  __shared__ __attribute__((aligned(16))) char lds[163840];
  asm volatile("" :: "n"(0), "s"((unsigned)(size_t)(__attribute__((address_space(3))) char*)lds));
  auto ka = __builtin_amdgcn_kernarg_segment_ptr();
  unsigned tid = __builtin_amdgcn_workitem_id_x();
  asm volatile(
    "s_load_dwordx16 s[4:19], s[0:1], 0x0\n"
    "s_load_dwordx16 s[20:35], s[0:1], 0x40\n"
    "s_load_dwordx4 s[36:39], s[0:1], 0x80\n"
    "v_lshrrev_b32 v3, 6, v0\n"
    "v_and_b32 v1, 63, v0\n"
    "v_readfirstlane_b32 s44, v3\n"
    "s_and_b32 s52, s2, 3\n"
    "s_bfe_u32 s53, s2, 0x20003\n"
    "s_lshl2_add_u32 s45, s52, s53\n"
    "s_bfe_u32 s52, s2, 0x10002\n"
    "s_lshr_b32 s54, s2, 5\n"
    "s_lshl3_add_u32 s46, s52, s54\n"
    "s_lshl1_add_u32 s64, s53, s54\n"
    "s_mul_i32 s52, s64, 37\n"
    "s_lshr_b32 s52, s52, 8\n"
    "s_mul_i32 s52, s52, 7\n"
    "s_sub_u32 s64, s64, s52\n"
    "v_lshrrev_b32 v8, 3, v1\n"
    "v_lshrrev_b32 v9, 4, v1\n"
    "v_and_b32 v10, 7, v1\n"
    "v_xor_b32 v11, v10, v9\n"
    "v_and_b32 v9, 1, v9\n"
    "v_lshlrev_b32 v9, 2, v9\n"
    "v_xor_b32 v10, v10, v9\n"
    "s_and_b32 s47, s44, 3\n"
    "s_mov_b32 s65, s64\n"
    "s_lshl_b32 s56, s45, 7\n"
    "s_lshr_b32 s67, 34956, s45\n"
    "s_and_b32 s67, s67, 1\n"
    "s_mov_b32 s50, 1233596944\n"
    "s_mov_b32 s51, 1877826746\n"
    "s_lshl_b32 s69, s45, 2\n"
    "s_lshr_b64 s[50:51], s[50:51], s69\n"
    "s_and_b32 s69, s50, 15\n"
    "s_cmp_ge_u32 s44, 4\n"
    "s_cbranch_scc1 .La_pro\n"
    "v_lshl_add_u32 v12, v3, 5, v8\n"
    "v_mul_u32_u24 v12, 896, v12\n"
    "v_lshl_add_u32 v192, v11, 4, v12\n"
    "v_xor_b32 v11, 4, v11\n"
    "v_lshl_add_u32 v193, v11, 4, v12\n"
    "v_add_u32 v193, 6144, v193\n"
    "v_add_u32 v194, 12288, v192\n"
    "v_add_u32 v195, 12288, v193\n"
    "v_lshl_add_u32 v13, v3, 4, v8\n"
    "v_mul_u32_u24 v12, 2688, v13\n"
    "v_lshl_add_u32 v196, v10, 4, v12\n"
    "v_xor_b32 v197, 32, v196\n"
    "v_add_u32 v197, 20480, v197\n"
    "s_mul_i32 s52, s46, 114688\n"
    "s_lshl_b32 s53, s69, 7\n"
    "s_add_u32 s53, s53, 640\n"
    "s_lshl_b32 s54, s44, 12\n"
    "s_lshl_b32 s55, s44, 11\n"
    "s_add_u32 m0, s54, 0\n"
    "s_lshl_b32 s57, s65, 7\n"
    "s_add_u32 s57, s57, s52\n"
    "s_waitcnt lgkmcnt(0)\n"
    "buffer_load_dwordx4 v192, s[4:7], s57 offen lds\n"
    "buffer_load_dwordx4 v193, s[4:7], s57 offen offset:1024 lds\n"
    "buffer_load_dwordx4 v194, s[4:7], s57 offen offset:2048 lds\n"
    "buffer_load_dwordx4 v195, s[4:7], s57 offen offset:3072 lds\n"
    "s_add_u32 m0, s55, 49152\n"
    "s_mul_i32 s57, s65, 172032\n"
    "s_add_u32 s57, s57, s53\n"
    "buffer_load_dwordx4 v196, s[8:11], s57 offen lds\n"
    "buffer_load_dwordx4 v197, s[8:11], s57 offen offset:1024 lds\n"
    "s_mov_b32 s50, 356713488\n"
    "s_mov_b32 s51, 1000941686\n"
    "s_lshl_b32 s66, s45, 2\n"
    "s_lshr_b64 s[50:51], s[50:51], s66\n"
    "s_and_b32 s66, s50, 15\n"
    "v_mul_u32_u24 v12, 3584, v13\n"
    "v_lshl_add_u32 v230, v10, 4, v12\n"
    "v_xor_b32 v231, 32, v230\n"
    "v_add_u32 v231, 27648, v231\n"
    "s_cmp_lg_u32 s67, 0\n"
    "s_cselect_b64 s[40:41], s[8:9], s[20:21]\n"
    "s_cselect_b64 s[42:43], s[10:11], s[22:23]\n"
    "s_cselect_b32 s73, 0, 16\n"
    "s_cselect_b64 vcc, -1, 0\n"
    "s_mov_b32 s72, 229376\n"
    "s_cmov_b32 s72, 172032\n"
    "s_add_u32 s66, s66, s73\n"
    "s_lshl_b32 s73, s66, 7\n"
    "v_cndmask_b32 v198, v230, v196, vcc\n"
    "v_cndmask_b32 v199, v231, v197, vcc\n"
    "s_add_u32 m0, s55, 73728\n"
    "s_mul_i32 s57, s65, s72\n"
    "s_add_u32 s57, s57, s73\n"
    "buffer_load_dwordx4 v198, s[40:43], s57 offen lds\n"
    "buffer_load_dwordx4 v199, s[40:43], s57 offen offset:1024 lds\n"
    "s_add_u32 s65, s65, 1\n"
    "s_cmp_eq_u32 s65, 7\n"
    "s_cselect_b32 s65, 0, s65\n"
    "v_lshlrev_b32 v2, 4, v1\n"
    "v_and_b32 v7, 31, v1\n"
    "v_lshrrev_b32 v3, 5, v1\n"
    "v_lshrrev_b32 v12, 4, v1\n"
    "v_bfe_u32 v13, v1, 2, 2\n"
    "v_lshl_add_u32 v6, v12, 3, v13\n"
    "v_and_b32 v12, 1, v12\n"
    "v_and_b32 v13, 2, v13\n"
    "v_or_b32 v12, v12, v13\n"
    "v_lshl_add_u32 v6, v6, 2, v12\n"
    "v_and_b32 v12, 3, v1\n"
    "v_lshl_add_u32 v6, v6, 2, v12\n"
    "v_lshlrev_b32 v6, 3, v6\n"
    "v_bfe_u32 v12, v1, 1, 3\n"
    "v_lshrrev_b32 v14, 4, v1\n"
    "v_xor_b32 v12, v12, v14\n"
    "s_lshl_b32 s57, s47, 12\n"
    "v_and_b32 v13, 15, v1\n"
    "v_lshl_add_u32 v13, v13, 7, s57\n"
    "v_lshl_add_u32 v8, v12, 4, v13\n"
    "v_xor_b32 v9, 64, v8\n"
    "s_lshl_b32 s57, s44, 2\n"
    "s_add_u32 s57, s57, 163328\n"
    "v_mov_b32 v0, s57\n"
    "v_mov_b32 v213, 0\n"
    "v_mov_b32 v217, 0\n"
    "v_mov_b32 v221, 0\n"
    "v_mov_b32 v214, 0\n"
    "v_mov_b32 v218, 0\n"
    "v_mov_b32 v222, 0\n"
    "v_mov_b32 v215, 0\n"
    "v_mov_b32 v219, 0\n"
    "v_mov_b32 v223, 0\n"
    "ds_write_b32 v0, v213\n"
    "ds_write_b32 v0, v213 offset:32\n"
    "v_mov_b32 v13, 0x3f80\n"
    "v_cmp_eq_u32 vcc, 0, v14\n"
    "s_nop 1\n"
    "v_cndmask_b32 v216, 0, v13, vcc\n"
    "v_cmp_eq_u32 vcc, 1, v14\n"
    "s_nop 1\n"
    "v_cndmask_b32 v220, 0, v13, vcc\n"
    "s_mov_b32 s0, 0x3fb8aa3b\n"
    "s_lshl2_add_u32 s62, s46, s47\n"
    "v_lshlrev_b32 v225, 1, v7\n"
    "v_xor_b32 v12, 73728, v6\n"
    "v_xor_b32 v13, 32, v12\n"
    "v_xor_b32 v14, 64, v12\n"
    "v_xor_b32 v15, 96, v12\n"
    "s_lshr_b32 s68, s45, 2\n"
    "s_xor_b32 s68, s68, s46\n"
    "s_lshl_b32 s68, s68, 2\n"
    "s_and_b32 s68, s68, 4\n"
    "s_and_b32 s57, s45, 3\n"
    "s_or_b32 s68, s68, s57\n"
    "s_add_u32 m0, s54, 16384\n"
    "s_lshl_b32 s57, s65, 7\n"
    "s_add_u32 s57, s57, s52\n"
    "buffer_load_dwordx4 v192, s[4:7], s57 offen lds\n"
    "buffer_load_dwordx4 v193, s[4:7], s57 offen offset:1024 lds\n"
    "buffer_load_dwordx4 v194, s[4:7], s57 offen offset:2048 lds\n"
    "buffer_load_dwordx4 v195, s[4:7], s57 offen offset:3072 lds\n"
    "s_add_u32 m0, s55, 57344\n"
    "s_mul_i32 s57, s65, 172032\n"
    "s_add_u32 s57, s57, s53\n"
    "buffer_load_dwordx4 v196, s[8:11], s57 offen lds\n"
    "buffer_load_dwordx4 v197, s[8:11], s57 offen offset:1024 lds\n"
    "s_add_u32 m0, s55, 81920\n"
    "s_mul_i32 s57, s65, s72\n"
    "s_add_u32 s57, s57, s73\n"
    "buffer_load_dwordx4 v198, s[40:43], s57 offen lds\n"
    "buffer_load_dwordx4 v199, s[40:43], s57 offen offset:1024 lds\n"
    "s_add_u32 s65, s65, 1\n"
    "s_cmp_eq_u32 s65, 7\n"
    "s_cselect_b32 s65, 0, s65\n"
    "s_setprio 2\n"
    "s_waitcnt vmcnt(8) lgkmcnt(0)\n"
    "s_barrier\n"
    "buffer_load_dwordx2 v[10:11], off, s[16:19], 0 sc1\n"
    "buffer_load_ushort v208, v225, s[24:27], s73 offen offset:0\n"
    "buffer_load_ushort v209, v225, s[24:27], s73 offen offset:64\n"
    "buffer_load_ushort v210, v225, s[24:27], s56 offen offset:64\n"
    "s_lshr_b32 s71, s45, 3\n"
    "s_lshl_b32 s57, s71, 6\n"
    "buffer_load_ushort v211, v225, s[24:27], s57 offen offset:2304\n"
    "s_add_u32 m0, s54, 32768\n"
    "s_lshl_b32 s57, s65, 7\n"
    "s_add_u32 s57, s57, s52\n"
    "buffer_load_dwordx4 v192, s[4:7], s57 offen lds\n"
    "ds_read_b128 v[64:67], v8 offset:0\n"
    "ds_read_b128 v[68:71], v8 offset:2048\n"
    "buffer_load_dwordx4 v193, s[4:7], s57 offen offset:1024 lds\n"
    "buffer_load_dwordx4 v194, s[4:7], s57 offen offset:2048 lds\n"
    "ds_read_b128 v[72:75], v9 offset:0\n"
    "ds_read_b128 v[76:79], v9 offset:2048\n"
    "ds_read_b64_tr_b16 v[32:33], v12 offset:0\n"
    "buffer_load_dwordx4 v195, s[4:7], s57 offen offset:3072 lds\n"
    "ds_read_b64_tr_b16 v[34:35], v12 offset:512\n"
    "ds_read_b64_tr_b16 v[36:37], v12 offset:4096\n"
    "ds_read_b64_tr_b16 v[38:39], v12 offset:4608\n"
    "s_add_u32 m0, s55, 65536\n"
    "s_mul_i32 s57, s65, 172032\n"
    "s_add_u32 s57, s57, s53\n"
    "buffer_load_dwordx4 v196, s[8:11], s57 offen lds\n"
    "ds_read_b64_tr_b16 v[40:41], v13 offset:0\n"
    "ds_read_b64_tr_b16 v[42:43], v13 offset:512\n"
    "ds_read_b64_tr_b16 v[44:45], v13 offset:4096\n"
    "buffer_load_dwordx4 v197, s[8:11], s57 offen offset:1024 lds\n"
    "ds_read_b64_tr_b16 v[46:47], v13 offset:4608\n"
    "ds_read_b64_tr_b16 v[48:49], v14 offset:0\n"
    "ds_read_b64_tr_b16 v[50:51], v14 offset:512\n"
    "ds_read_b64_tr_b16 v[52:53], v14 offset:4096\n"
    "s_add_u32 m0, s55, 90112\n"
    "s_mul_i32 s57, s65, s72\n"
    "s_add_u32 s57, s57, s73\n"
    "buffer_load_dwordx4 v198, s[40:43], s57 offen lds\n"
    "ds_read_b64_tr_b16 v[54:55], v14 offset:4608\n"
    "ds_read_b64_tr_b16 v[56:57], v15 offset:0\n"
    "ds_read_b64_tr_b16 v[58:59], v15 offset:512\n"
    "ds_read_b64_tr_b16 v[60:61], v15 offset:4096\n"
    "s_waitcnt lgkmcnt(11)\n"
    "v_mfma_f32_16x16x32_bf16 v[16:19], v[32:35], v[64:67], 0\n"
    "ds_read_b64_tr_b16 v[62:63], v15 offset:4608\n"
    "v_mfma_f32_16x16x32_bf16 v[16:19], v[36:39], v[72:75], v[16:19]\n"
    "buffer_load_dwordx4 v199, s[40:43], s57 offen offset:1024 lds\n"
    "s_add_u32 s65, s65, 1\n"
    "s_cmp_eq_u32 s65, 7\n"
    "s_cselect_b32 s65, 0, s65\n"
    "v_mfma_f32_16x16x32_bf16 v[24:27], v[32:35], v[68:71], 0\n"
    "v_mfma_f32_16x16x32_bf16 v[24:27], v[36:39], v[76:79], v[24:27]\n"
    "s_waitcnt vmcnt(13) lgkmcnt(0)\n"
    "s_barrier\n"
    "v_mfma_f32_16x16x32_bf16 v[20:23], v[40:43], v[64:67], 0\n"
    "v_mfma_f32_16x16x32_bf16 v[20:23], v[44:47], v[72:75], v[20:23]\n"
    "s_add_u32 m0, s54, 0\n"
    "s_lshl_b32 s57, s65, 7\n"
    "s_add_u32 s57, s57, s52\n"
    "buffer_load_dwordx4 v192, s[4:7], s57 offen lds\n"
    "buffer_load_dwordx4 v193, s[4:7], s57 offen offset:1024 lds\n"
    "ds_read_b128 v[80:83], v8 offset:16384\n"
    "ds_read_b128 v[84:87], v8 offset:18432\n"
    "v_mfma_f32_16x16x32_bf16 v[28:31], v[40:43], v[68:71], 0\n"
    "ds_read_b128 v[88:91], v9 offset:16384\n"
    "v_mfma_f32_16x16x32_bf16 v[28:31], v[44:47], v[76:79], v[28:31]\n"
    "buffer_load_dwordx4 v194, s[4:7], s57 offen offset:2048 lds\n"
    "ds_read_b128 v[92:95], v9 offset:18432\n"
    "ds_read_b64_tr_b16 v[32:33], v12 offset:8192\n"
    "v_mfma_f32_16x16x32_bf16 v[240:243], v[48:51], v[64:67], 0\n"
    "ds_read_b64_tr_b16 v[34:35], v12 offset:8704\n"
    "v_mfma_f32_16x16x32_bf16 v[240:243], v[52:55], v[72:75], v[240:243]\n"
    "buffer_load_dwordx4 v195, s[4:7], s57 offen offset:3072 lds\n"
    "ds_read_b64_tr_b16 v[36:37], v12 offset:12288\n"
    "ds_read_b64_tr_b16 v[38:39], v12 offset:12800\n"
    "v_mfma_f32_16x16x32_bf16 v[248:251], v[48:51], v[68:71], 0\n"
    "ds_read_b64_tr_b16 v[40:41], v13 offset:8192\n"
    "v_mfma_f32_16x16x32_bf16 v[248:251], v[52:55], v[76:79], v[248:251]\n"
    "s_add_u32 m0, s55, 49152\n"
    "s_mul_i32 s57, s65, 172032\n"
    "s_add_u32 s57, s57, s53\n"
    "buffer_load_dwordx4 v196, s[8:11], s57 offen lds\n"
    "ds_read_b64_tr_b16 v[42:43], v13 offset:8704\n"
    "ds_read_b64_tr_b16 v[44:45], v13 offset:12288\n"
    "v_mfma_f32_16x16x32_bf16 v[244:247], v[56:59], v[64:67], 0\n"
    "ds_read_b64_tr_b16 v[46:47], v13 offset:12800\n"
    "v_mfma_f32_16x16x32_bf16 v[244:247], v[60:63], v[72:75], v[244:247]\n"
    "buffer_load_dwordx4 v197, s[8:11], s57 offen offset:1024 lds\n"
    "ds_read_b64_tr_b16 v[48:49], v14 offset:8192\n"
    "ds_read_b64_tr_b16 v[50:51], v14 offset:8704\n"
    "ds_read_b64_tr_b16 v[52:53], v14 offset:12288\n"
    "v_mfma_f32_16x16x32_bf16 v[252:255], v[56:59], v[68:71], 0\n"
    "ds_read_b64_tr_b16 v[54:55], v14 offset:12800\n"
    "v_mfma_f32_16x16x32_bf16 v[252:255], v[60:63], v[76:79], v[252:255]\n"
    "s_add_u32 m0, s55, 73728\n"
    "s_mul_i32 s57, s65, s72\n"
    "s_add_u32 s57, s57, s73\n"
    "buffer_load_dwordx4 v198, s[40:43], s57 offen lds\n"
    "ds_read_b64_tr_b16 v[56:57], v15 offset:8192\n"
    "ds_read_b64_tr_b16 v[58:59], v15 offset:8704\n"
    "ds_read_b64_tr_b16 v[60:61], v15 offset:12288\n"
    "s_waitcnt lgkmcnt(11)\n"
    "v_mfma_f32_16x16x32_bf16 v[16:19], v[32:35], v[80:83], v[16:19]\n"
    "ds_read_b64_tr_b16 v[62:63], v15 offset:12800\n"
    "v_mfma_f32_16x16x32_bf16 v[16:19], v[36:39], v[88:91], v[16:19]\n"
    "buffer_load_dwordx4 v199, s[40:43], s57 offen offset:1024 lds\n"
    "s_add_u32 s65, s65, 1\n"
    "s_cmp_eq_u32 s65, 7\n"
    "s_cselect_b32 s65, 0, s65\n"
    "v_mfma_f32_16x16x32_bf16 v[24:27], v[32:35], v[84:87], v[24:27]\n"
    "v_mfma_f32_16x16x32_bf16 v[24:27], v[36:39], v[92:95], v[24:27]\n"
    "s_waitcnt vmcnt(8) lgkmcnt(0)\n"
    "s_barrier\n"
    "v_mfma_f32_16x16x32_bf16 v[20:23], v[40:43], v[80:83], v[20:23]\n"
    "v_mfma_f32_16x16x32_bf16 v[20:23], v[44:47], v[88:91], v[20:23]\n"
    "s_add_u32 m0, s54, 16384\n"
    "s_lshl_b32 s57, s65, 7\n"
    "s_add_u32 s57, s57, s52\n"
    "buffer_load_dwordx4 v192, s[4:7], s57 offen lds\n"
    "buffer_load_dwordx4 v193, s[4:7], s57 offen offset:1024 lds\n"
    "ds_read_b128 v[96:99], v8 offset:32768\n"
    "ds_read_b128 v[100:103], v8 offset:34816\n"
    "v_mfma_f32_16x16x32_bf16 v[28:31], v[40:43], v[84:87], v[28:31]\n"
    "ds_read_b128 v[104:107], v9 offset:32768\n"
    "v_mfma_f32_16x16x32_bf16 v[28:31], v[44:47], v[92:95], v[28:31]\n"
    "buffer_load_dwordx4 v194, s[4:7], s57 offen offset:2048 lds\n"
    "ds_read_b128 v[108:111], v9 offset:34816\n"
    "ds_read_b64_tr_b16 v[32:33], v12 offset:16384\n"
    "v_mfma_f32_16x16x32_bf16 v[240:243], v[48:51], v[80:83], v[240:243]\n"
    "ds_read_b64_tr_b16 v[34:35], v12 offset:16896\n"
    "v_mfma_f32_16x16x32_bf16 v[240:243], v[52:55], v[88:91], v[240:243]\n"
    "buffer_load_dwordx4 v195, s[4:7], s57 offen offset:3072 lds\n"
    "ds_read_b64_tr_b16 v[36:37], v12 offset:20480\n"
    "ds_read_b64_tr_b16 v[38:39], v12 offset:20992\n"
    "v_mfma_f32_16x16x32_bf16 v[248:251], v[48:51], v[84:87], v[248:251]\n"
    "ds_read_b64_tr_b16 v[40:41], v13 offset:16384\n"
    "v_mfma_f32_16x16x32_bf16 v[248:251], v[52:55], v[92:95], v[248:251]\n"
    "s_add_u32 m0, s55, 57344\n"
    "s_mul_i32 s57, s65, 172032\n"
    "s_add_u32 s57, s57, s53\n"
    "buffer_load_dwordx4 v196, s[8:11], s57 offen lds\n"
    "ds_read_b64_tr_b16 v[42:43], v13 offset:16896\n"
    "ds_read_b64_tr_b16 v[44:45], v13 offset:20480\n"
    "v_mfma_f32_16x16x32_bf16 v[244:247], v[56:59], v[80:83], v[244:247]\n"
    "ds_read_b64_tr_b16 v[46:47], v13 offset:20992\n"
    "v_mfma_f32_16x16x32_bf16 v[244:247], v[60:63], v[88:91], v[244:247]\n"
    "buffer_load_dwordx4 v197, s[8:11], s57 offen offset:1024 lds\n"
    "ds_read_b64_tr_b16 v[48:49], v14 offset:16384\n"
    "ds_read_b64_tr_b16 v[50:51], v14 offset:16896\n"
    "ds_read_b64_tr_b16 v[52:53], v14 offset:20480\n"
    "v_mfma_f32_16x16x32_bf16 v[252:255], v[56:59], v[84:87], v[252:255]\n"
    "ds_read_b64_tr_b16 v[54:55], v14 offset:20992\n"
    "v_mfma_f32_16x16x32_bf16 v[252:255], v[60:63], v[92:95], v[252:255]\n"
    "s_add_u32 m0, s55, 81920\n"
    "s_mul_i32 s57, s65, s72\n"
    "s_add_u32 s57, s57, s73\n"
    "buffer_load_dwordx4 v198, s[40:43], s57 offen lds\n"
    "ds_read_b64_tr_b16 v[56:57], v15 offset:16384\n"
    "ds_read_b64_tr_b16 v[58:59], v15 offset:16896\n"
    "ds_read_b64_tr_b16 v[60:61], v15 offset:20480\n"
    "s_waitcnt lgkmcnt(11)\n"
    "v_mfma_f32_16x16x32_bf16 v[16:19], v[32:35], v[96:99], v[16:19]\n"
    "ds_read_b64_tr_b16 v[62:63], v15 offset:20992\n"
    "v_mfma_f32_16x16x32_bf16 v[16:19], v[36:39], v[104:107], v[16:19]\n"
    "buffer_load_dwordx4 v199, s[40:43], s57 offen offset:1024 lds\n"
    "s_add_u32 s65, s65, 1\n"
    "s_cmp_eq_u32 s65, 7\n"
    "s_cselect_b32 s65, 0, s65\n"
    "v_mfma_f32_16x16x32_bf16 v[24:27], v[32:35], v[100:103], v[24:27]\n"
    "v_mfma_f32_16x16x32_bf16 v[24:27], v[36:39], v[108:111], v[24:27]\n"
    "s_waitcnt vmcnt(8) lgkmcnt(0)\n"
    "v_readfirstlane_b32 s48, v10\n"
    "v_readfirstlane_b32 s49, v11\n"
    "s_add_u32 s48, s48, 0x7f4a7c15\n"
    "s_addc_u32 s49, s49, 0x9e3779b9\n"
    "s_barrier\n"
    "v_mfma_f32_16x16x32_bf16 v[20:23], v[40:43], v[96:99], v[20:23]\n"
    "v_mfma_f32_16x16x32_bf16 v[20:23], v[44:47], v[104:107], v[20:23]\n"
    "s_add_u32 m0, s54, 32768\n"
    "s_lshl_b32 s57, s65, 7\n"
    "s_add_u32 s57, s57, s52\n"
    "buffer_load_dwordx4 v192, s[4:7], s57 offen lds\n"
    "buffer_load_dwordx4 v193, s[4:7], s57 offen offset:1024 lds\n"
    "ds_read_b128 v[112:115], v8 offset:0\n"
    "ds_read_b128 v[116:119], v8 offset:2048\n"
    "v_mfma_f32_16x16x32_bf16 v[28:31], v[40:43], v[100:103], v[28:31]\n"
    "ds_read_b128 v[120:123], v9 offset:0\n"
    "v_mfma_f32_16x16x32_bf16 v[28:31], v[44:47], v[108:111], v[28:31]\n"
    "buffer_load_dwordx4 v194, s[4:7], s57 offen offset:2048 lds\n"
    "ds_read_b128 v[124:127], v9 offset:2048\n"
    "ds_read_b64_tr_b16 v[32:33], v12 offset:0\n"
    "v_mfma_f32_16x16x32_bf16 v[240:243], v[48:51], v[96:99], v[240:243]\n"
    "ds_read_b64_tr_b16 v[34:35], v12 offset:512\n"
    "v_mfma_f32_16x16x32_bf16 v[240:243], v[52:55], v[104:107], v[240:243]\n"
    "buffer_load_dwordx4 v195, s[4:7], s57 offen offset:3072 lds\n"
    "ds_read_b64_tr_b16 v[36:37], v12 offset:4096\n"
    "ds_read_b64_tr_b16 v[38:39], v12 offset:4608\n"
    "v_mfma_f32_16x16x32_bf16 v[248:251], v[48:51], v[100:103], v[248:251]\n"
    "ds_read_b64_tr_b16 v[40:41], v13 offset:0\n"
    "v_mfma_f32_16x16x32_bf16 v[248:251], v[52:55], v[108:111], v[248:251]\n"
    "s_add_u32 m0, s55, 65536\n"
    "s_mul_i32 s57, s65, 172032\n"
    "s_add_u32 s57, s57, s53\n"
    "buffer_load_dwordx4 v196, s[8:11], s57 offen lds\n"
    "ds_read_b64_tr_b16 v[42:43], v13 offset:512\n"
    "ds_read_b64_tr_b16 v[44:45], v13 offset:4096\n"
    "v_mfma_f32_16x16x32_bf16 v[244:247], v[56:59], v[96:99], v[244:247]\n"
    "ds_read_b64_tr_b16 v[46:47], v13 offset:4608\n"
    "v_mfma_f32_16x16x32_bf16 v[244:247], v[60:63], v[104:107], v[244:247]\n"
    "buffer_load_dwordx4 v197, s[8:11], s57 offen offset:1024 lds\n"
    "ds_read_b64_tr_b16 v[48:49], v14 offset:0\n"
    "ds_read_b64_tr_b16 v[50:51], v14 offset:512\n"
    "ds_read_b64_tr_b16 v[52:53], v14 offset:4096\n"
    "v_mfma_f32_16x16x32_bf16 v[252:255], v[56:59], v[100:103], v[252:255]\n"
    "ds_read_b64_tr_b16 v[54:55], v14 offset:4608\n"
    "v_mfma_f32_16x16x32_bf16 v[252:255], v[60:63], v[108:111], v[252:255]\n"
    "s_add_u32 m0, s55, 90112\n"
    "s_mul_i32 s57, s65, s72\n"
    "s_add_u32 s57, s57, s73\n"
    "buffer_load_dwordx4 v198, s[40:43], s57 offen lds\n"
    "ds_read_b64_tr_b16 v[56:57], v15 offset:0\n"
    "ds_read_b64_tr_b16 v[58:59], v15 offset:512\n"
    "ds_read_b64_tr_b16 v[60:61], v15 offset:4096\n"
    "s_waitcnt lgkmcnt(11)\n"
    "v_mfma_f32_16x16x32_bf16 v[16:19], v[32:35], v[112:115], v[16:19]\n"
    "ds_read_b64_tr_b16 v[62:63], v15 offset:4608\n"
    "v_mfma_f32_16x16x32_bf16 v[16:19], v[36:39], v[120:123], v[16:19]\n"
    "buffer_load_dwordx4 v199, s[40:43], s57 offen offset:1024 lds\n"
    "s_add_u32 s65, s65, 1\n"
    "s_cmp_eq_u32 s65, 7\n"
    "s_cselect_b32 s65, 0, s65\n"
    "v_mfma_f32_16x16x32_bf16 v[24:27], v[32:35], v[116:119], v[24:27]\n"
    "v_mfma_f32_16x16x32_bf16 v[24:27], v[36:39], v[124:127], v[24:27]\n"
    "s_waitcnt vmcnt(8) lgkmcnt(0)\n"
    "s_barrier\n"
    "v_mfma_f32_16x16x32_bf16 v[20:23], v[40:43], v[112:115], v[20:23]\n"
    "v_mfma_f32_16x16x32_bf16 v[20:23], v[44:47], v[120:123], v[20:23]\n"
    "s_add_u32 m0, s54, 0\n"
    "s_lshl_b32 s57, s65, 7\n"
    "s_add_u32 s57, s57, s52\n"
    "buffer_load_dwordx4 v192, s[4:7], s57 offen lds\n"
    "buffer_load_dwordx4 v193, s[4:7], s57 offen offset:1024 lds\n"
    "ds_read_b128 v[128:131], v8 offset:16384\n"
    "ds_read_b128 v[132:135], v8 offset:18432\n"
    "v_mfma_f32_16x16x32_bf16 v[28:31], v[40:43], v[116:119], v[28:31]\n"
    "ds_read_b128 v[136:139], v9 offset:16384\n"
    "v_mfma_f32_16x16x32_bf16 v[28:31], v[44:47], v[124:127], v[28:31]\n"
    "buffer_load_dwordx4 v194, s[4:7], s57 offen offset:2048 lds\n"
    "ds_read_b128 v[140:143], v9 offset:18432\n"
    "ds_read_b64_tr_b16 v[32:33], v12 offset:8192\n"
    "v_mfma_f32_16x16x32_bf16 v[240:243], v[48:51], v[112:115], v[240:243]\n"
    "ds_read_b64_tr_b16 v[34:35], v12 offset:8704\n"
    "v_mfma_f32_16x16x32_bf16 v[240:243], v[52:55], v[120:123], v[240:243]\n"
    "buffer_load_dwordx4 v195, s[4:7], s57 offen offset:3072 lds\n"
    "ds_read_b64_tr_b16 v[36:37], v12 offset:12288\n"
    "ds_read_b64_tr_b16 v[38:39], v12 offset:12800\n"
    "v_mfma_f32_16x16x32_bf16 v[248:251], v[48:51], v[116:119], v[248:251]\n"
    "ds_read_b64_tr_b16 v[40:41], v13 offset:8192\n"
    "v_mfma_f32_16x16x32_bf16 v[248:251], v[52:55], v[124:127], v[248:251]\n"
    "s_add_u32 m0, s55, 49152\n"
    "s_mul_i32 s57, s65, 172032\n"
    "s_add_u32 s57, s57, s53\n"
    "buffer_load_dwordx4 v196, s[8:11], s57 offen lds\n"
    "ds_read_b64_tr_b16 v[42:43], v13 offset:8704\n"
    "ds_read_b64_tr_b16 v[44:45], v13 offset:12288\n"
    "v_mfma_f32_16x16x32_bf16 v[244:247], v[56:59], v[112:115], v[244:247]\n"
    "ds_read_b64_tr_b16 v[46:47], v13 offset:12800\n"
    "v_mfma_f32_16x16x32_bf16 v[244:247], v[60:63], v[120:123], v[244:247]\n"
    "buffer_load_dwordx4 v197, s[8:11], s57 offen offset:1024 lds\n"
    "ds_read_b64_tr_b16 v[48:49], v14 offset:8192\n"
    "ds_read_b64_tr_b16 v[50:51], v14 offset:8704\n"
    "ds_read_b64_tr_b16 v[52:53], v14 offset:12288\n"
    "v_mfma_f32_16x16x32_bf16 v[252:255], v[56:59], v[116:119], v[252:255]\n"
    "ds_read_b64_tr_b16 v[54:55], v14 offset:12800\n"
    "v_mfma_f32_16x16x32_bf16 v[252:255], v[60:63], v[124:127], v[252:255]\n"
    "s_add_u32 m0, s55, 73728\n"
    "s_mul_i32 s57, s65, s72\n"
    "s_add_u32 s57, s57, s73\n"
    "buffer_load_dwordx4 v198, s[40:43], s57 offen lds\n"
    "ds_read_b64_tr_b16 v[56:57], v15 offset:8192\n"
    "ds_read_b64_tr_b16 v[58:59], v15 offset:8704\n"
    "ds_read_b64_tr_b16 v[60:61], v15 offset:12288\n"
    "s_waitcnt lgkmcnt(11)\n"
    "v_mfma_f32_16x16x32_bf16 v[16:19], v[32:35], v[128:131], v[16:19]\n"
    "ds_read_b64_tr_b16 v[62:63], v15 offset:12800\n"
    "v_mfma_f32_16x16x32_bf16 v[16:19], v[36:39], v[136:139], v[16:19]\n"
    "buffer_load_dwordx4 v199, s[40:43], s57 offen offset:1024 lds\n"
    "s_add_u32 s65, s65, 1\n"
    "s_cmp_eq_u32 s65, 7\n"
    "s_cselect_b32 s65, 0, s65\n"
    "v_mfma_f32_16x16x32_bf16 v[24:27], v[32:35], v[132:135], v[24:27]\n"
    "v_mfma_f32_16x16x32_bf16 v[24:27], v[36:39], v[140:143], v[24:27]\n"
    "s_waitcnt vmcnt(8) lgkmcnt(0)\n"
    "s_barrier\n"
    "v_mfma_f32_16x16x32_bf16 v[20:23], v[40:43], v[128:131], v[20:23]\n"
    "v_mfma_f32_16x16x32_bf16 v[20:23], v[44:47], v[136:139], v[20:23]\n"
    "s_add_u32 m0, s55, 98304\n"
    "s_mul_i32 s57, s64, 229376\n"
    "s_add_u32 s57, s57, s56\n"
    "buffer_load_dwordx4 v230, s[20:23], s57 offen lds\n"
    "buffer_load_dwordx4 v231, s[20:23], s57 offen offset:1024 lds\n"
    "s_add_u32 s64, s64, 1\n"
    "s_cmp_eq_u32 s64, 7\n"
    "s_cselect_b32 s64, 0, s64\n"
    "ds_read_b128 v[144:147], v8 offset:32768\n"
    "ds_read_b128 v[148:151], v8 offset:34816\n"
    "v_mfma_f32_16x16x32_bf16 v[28:31], v[40:43], v[132:135], v[28:31]\n"
    "ds_read_b128 v[152:155], v9 offset:32768\n"
    "v_mfma_f32_16x16x32_bf16 v[28:31], v[44:47], v[140:143], v[28:31]\n"
    "s_add_u32 m0, s55, 106496\n"
    "s_mul_i32 s57, s64, 229376\n"
    "s_add_u32 s57, s57, s56\n"
    "buffer_load_dwordx4 v230, s[20:23], s57 offen lds\n"
    "ds_read_b128 v[156:159], v9 offset:34816\n"
    "ds_read_b64_tr_b16 v[32:33], v12 offset:16384\n"
    "v_mfma_f32_16x16x32_bf16 v[240:243], v[48:51], v[128:131], v[240:243]\n"
    "ds_read_b64_tr_b16 v[34:35], v12 offset:16896\n"
    "v_mfma_f32_16x16x32_bf16 v[240:243], v[52:55], v[136:139], v[240:243]\n"
    "buffer_load_dwordx4 v231, s[20:23], s57 offen offset:1024 lds\n"
    "s_add_u32 s64, s64, 1\n"
    "s_cmp_eq_u32 s64, 7\n"
    "s_cselect_b32 s64, 0, s64\n"
    "ds_read_b64_tr_b16 v[36:37], v12 offset:20480\n"
    "ds_read_b64_tr_b16 v[38:39], v12 offset:20992\n"
    "v_mfma_f32_16x16x32_bf16 v[248:251], v[48:51], v[132:135], v[248:251]\n"
    "ds_read_b64_tr_b16 v[40:41], v13 offset:16384\n"
    "v_mfma_f32_16x16x32_bf16 v[248:251], v[52:55], v[140:143], v[248:251]\n"
    "s_add_u32 m0, s55, 114688\n"
    "s_mul_i32 s57, s64, 229376\n"
    "s_add_u32 s57, s57, s56\n"
    "buffer_load_dwordx4 v230, s[20:23], s57 offen lds\n"
    "ds_read_b64_tr_b16 v[42:43], v13 offset:16896\n"
    "ds_read_b64_tr_b16 v[44:45], v13 offset:20480\n"
    "v_mfma_f32_16x16x32_bf16 v[244:247], v[56:59], v[128:131], v[244:247]\n"
    "ds_read_b64_tr_b16 v[46:47], v13 offset:20992\n"
    "v_mfma_f32_16x16x32_bf16 v[244:247], v[60:63], v[136:139], v[244:247]\n"
    "buffer_load_dwordx4 v231, s[20:23], s57 offen offset:1024 lds\n"
    "s_add_u32 s64, s64, 1\n"
    "s_cmp_eq_u32 s64, 7\n"
    "s_cselect_b32 s64, 0, s64\n"
    "ds_read_b64_tr_b16 v[48:49], v14 offset:16384\n"
    "ds_read_b64_tr_b16 v[50:51], v14 offset:16896\n"
    "ds_read_b64_tr_b16 v[52:53], v14 offset:20480\n"
    "v_mfma_f32_16x16x32_bf16 v[252:255], v[56:59], v[132:135], v[252:255]\n"
    "ds_read_b64_tr_b16 v[54:55], v14 offset:20992\n"
    "v_mfma_f32_16x16x32_bf16 v[252:255], v[60:63], v[140:143], v[252:255]\n"
    "s_add_u32 m0, s55, 122880\n"
    "s_mul_i32 s57, s64, 229376\n"
    "s_add_u32 s57, s57, s56\n"
    "buffer_load_dwordx4 v230, s[20:23], s57 offen lds\n"
    "ds_read_b64_tr_b16 v[56:57], v15 offset:16384\n"
    "ds_read_b64_tr_b16 v[58:59], v15 offset:16896\n"
    "ds_read_b64_tr_b16 v[60:61], v15 offset:20480\n"
    "s_waitcnt lgkmcnt(11)\n"
    "v_mfma_f32_16x16x32_bf16 v[16:19], v[32:35], v[144:147], v[16:19]\n"
    "ds_read_b64_tr_b16 v[62:63], v15 offset:20992\n"
    "v_mfma_f32_16x16x32_bf16 v[16:19], v[36:39], v[152:155], v[16:19]\n"
    "buffer_load_dwordx4 v231, s[20:23], s57 offen offset:1024 lds\n"
    "s_add_u32 s64, s64, 1\n"
    "s_cmp_eq_u32 s64, 7\n"
    "s_cselect_b32 s64, 0, s64\n"
    "v_mfma_f32_16x16x32_bf16 v[24:27], v[32:35], v[148:151], v[24:27]\n"
    "v_mfma_f32_16x16x32_bf16 v[24:27], v[36:39], v[156:159], v[24:27]\n"
    "s_waitcnt vmcnt(8) lgkmcnt(0)\n"
    "s_barrier\n"
    "v_mfma_f32_16x16x32_bf16 v[20:23], v[40:43], v[144:147], v[20:23]\n"
    "v_mfma_f32_16x16x32_bf16 v[20:23], v[44:47], v[152:155], v[20:23]\n"
    "s_add_u32 m0, s55, 131072\n"
    "s_mul_i32 s57, s64, 229376\n"
    "s_add_u32 s57, s57, s56\n"
    "buffer_load_dwordx4 v230, s[20:23], s57 offen lds\n"
    "ds_read_b128 v[160:163], v8 offset:0\n"
    "ds_read_b128 v[164:167], v8 offset:2048\n"
    "v_mfma_f32_16x16x32_bf16 v[28:31], v[40:43], v[148:151], v[28:31]\n"
    "ds_read_b128 v[168:171], v9 offset:0\n"
    "v_mfma_f32_16x16x32_bf16 v[28:31], v[44:47], v[156:159], v[28:31]\n"
    "buffer_load_dwordx4 v231, s[20:23], s57 offen offset:1024 lds\n"
    "s_add_u32 s64, s64, 1\n"
    "s_cmp_eq_u32 s64, 7\n"
    "s_cselect_b32 s64, 0, s64\n"
    "ds_read_b128 v[172:175], v9 offset:2048\n"
    "ds_read_b64_tr_b16 v[32:33], v12 offset:0\n"
    "v_mfma_f32_16x16x32_bf16 v[240:243], v[48:51], v[144:147], v[240:243]\n"
    "ds_read_b64_tr_b16 v[34:35], v12 offset:512\n"
    "v_mfma_f32_16x16x32_bf16 v[240:243], v[52:55], v[152:155], v[240:243]\n"
    "s_add_u32 m0, s55, 139264\n"
    "s_mul_i32 s57, s64, 229376\n"
    "s_add_u32 s57, s57, s56\n"
    "buffer_load_dwordx4 v230, s[20:23], s57 offen lds\n"
    "ds_read_b64_tr_b16 v[36:37], v12 offset:4096\n"
    "ds_read_b64_tr_b16 v[38:39], v12 offset:4608\n"
    "v_mfma_f32_16x16x32_bf16 v[248:251], v[48:51], v[148:151], v[248:251]\n"
    "ds_read_b64_tr_b16 v[40:41], v13 offset:0\n"
    "v_mfma_f32_16x16x32_bf16 v[248:251], v[52:55], v[156:159], v[248:251]\n"
    "buffer_load_dwordx4 v231, s[20:23], s57 offen offset:1024 lds\n"
    "s_add_u32 s64, s64, 1\n"
    "s_cmp_eq_u32 s64, 7\n"
    "s_cselect_b32 s64, 0, s64\n"
    "ds_read_b64_tr_b16 v[42:43], v13 offset:512\n"
    "ds_read_b64_tr_b16 v[44:45], v13 offset:4096\n"
    "v_mfma_f32_16x16x32_bf16 v[244:247], v[56:59], v[144:147], v[244:247]\n"
    "ds_read_b64_tr_b16 v[46:47], v13 offset:4608\n"
    "v_mfma_f32_16x16x32_bf16 v[244:247], v[60:63], v[152:155], v[244:247]\n"
    "s_add_u32 m0, s55, 147456\n"
    "s_mul_i32 s57, s64, 229376\n"
    "s_add_u32 s57, s57, s56\n"
    "buffer_load_dwordx4 v230, s[20:23], s57 offen lds\n"
    "ds_read_b64_tr_b16 v[48:49], v14 offset:0\n"
    "ds_read_b64_tr_b16 v[50:51], v14 offset:512\n"
    "ds_read_b64_tr_b16 v[52:53], v14 offset:4096\n"
    "v_mfma_f32_16x16x32_bf16 v[252:255], v[56:59], v[148:151], v[252:255]\n"
    "ds_read_b64_tr_b16 v[54:55], v14 offset:4608\n"
    "v_mfma_f32_16x16x32_bf16 v[252:255], v[60:63], v[156:159], v[252:255]\n"
    "buffer_load_dwordx4 v231, s[20:23], s57 offen offset:1024 lds\n"
    "s_add_u32 s64, s64, 1\n"
    "s_cmp_eq_u32 s64, 7\n"
    "s_cselect_b32 s64, 0, s64\n"
    "ds_read_b64_tr_b16 v[56:57], v15 offset:0\n"
    "ds_read_b64_tr_b16 v[58:59], v15 offset:512\n"
    "ds_read_b64_tr_b16 v[60:61], v15 offset:4096\n"
    "s_waitcnt lgkmcnt(11)\n"
    "v_mfma_f32_16x16x32_bf16 v[16:19], v[32:35], v[160:163], v[16:19]\n"
    "ds_read_b64_tr_b16 v[62:63], v15 offset:4608\n"
    "v_mfma_f32_16x16x32_bf16 v[16:19], v[36:39], v[168:171], v[16:19]\n"
    "v_mfma_f32_16x16x32_bf16 v[24:27], v[32:35], v[164:167], v[24:27]\n"
    "v_mfma_f32_16x16x32_bf16 v[24:27], v[36:39], v[172:175], v[24:27]\n"
    "s_waitcnt vmcnt(0) lgkmcnt(0)\n"
    "s_barrier\n"
    "v_mfma_f32_16x16x32_bf16 v[20:23], v[40:43], v[160:163], v[20:23]\n"
    "v_mfma_f32_16x16x32_bf16 v[20:23], v[44:47], v[168:171], v[20:23]\n"
    "v_mfma_f32_16x16x32_bf16 v[28:31], v[40:43], v[164:167], v[28:31]\n"
    "v_mfma_f32_16x16x32_bf16 v[28:31], v[44:47], v[172:175], v[28:31]\n"
    "v_mfma_f32_16x16x32_bf16 v[240:243], v[48:51], v[160:163], v[240:243]\n"
    "v_mfma_f32_16x16x32_bf16 v[240:243], v[52:55], v[168:171], v[240:243]\n"
    "v_mfma_f32_16x16x32_bf16 v[248:251], v[48:51], v[164:167], v[248:251]\n"
    "v_mfma_f32_16x16x32_bf16 v[248:251], v[52:55], v[172:175], v[248:251]\n"
    "v_mfma_f32_16x16x32_bf16 v[244:247], v[56:59], v[160:163], v[244:247]\n"
    "v_mfma_f32_16x16x32_bf16 v[244:247], v[60:63], v[168:171], v[244:247]\n"
    "v_mfma_f32_16x16x32_bf16 v[252:255], v[56:59], v[164:167], v[252:255]\n"
    "v_mfma_f32_16x16x32_bf16 v[252:255], v[60:63], v[172:175], v[252:255]\n"
    "s_cmp_lg_u32 s67, 0\n"
    "s_cbranch_scc1 .Lf_q\n"
    "v_mov_b32 v212, v208\n"
    "s_nop 1\n"
    "v_mfma_f32_16x16x32_bf16 v[16:19], v[212:215], v[216:219], v[16:19]\n"
    "v_mfma_f32_16x16x32_bf16 v[20:23], v[212:215], v[220:223], v[20:23]\n"
    "v_mfma_f32_16x16x32_bf16 v[24:27], v[212:215], v[216:219], v[24:27]\n"
    "v_mfma_f32_16x16x32_bf16 v[28:31], v[212:215], v[220:223], v[28:31]\n"
    "v_mov_b32 v212, v209\n"
    "s_nop 1\n"
    "v_mfma_f32_16x16x32_bf16 v[240:243], v[212:215], v[216:219], v[240:243]\n"
    "v_mfma_f32_16x16x32_bf16 v[244:247], v[212:215], v[220:223], v[244:247]\n"
    "v_mfma_f32_16x16x32_bf16 v[248:251], v[212:215], v[216:219], v[248:251]\n"
    "v_mfma_f32_16x16x32_bf16 v[252:255], v[212:215], v[220:223], v[252:255]\n"
    "s_lshl_b32 s52, s66, 1\n"
    "s_mul_i32 s71, s62, 56\n"
    "s_add_u32 s71, s71, s52\n"
    "s_lshl_b32 s71, s71, 11\n"
    "s_add_u32 s71, s71, 7340032\n"
    "s_setprio 1\n"
    "s_mov_b32 s75, 4\n"
    "s_call_b64 s[60:61], .Lfmish\n"
    "s_lshl2_add_u32 s52, s2, s47\n"
    "s_mul_i32 s53, s52, 1600\n"
    "s_mul_i32 s52, s52, 448\n"
    "v_lshlrev_b32 v225, 6, v1\n"
    "s_mov_b64 exec, 0x1ffffff\n"
    "buffer_load_dword v208, v225, s[28:31], s53 offen\n"
    "s_mov_b64 exec, 0x7f\n"
    "buffer_load_dword v209, v225, s[32:35], s52 offen\n"
    "s_mov_b64 exec, -1\n"
    "s_branch .Lf_post\n"
    ".Lf_q:\n"
    "s_mul_i32 s52, s62, 7\n"
    "s_add_u32 s52, s52, s66\n"
    "s_lshl_b32 s52, s52, 12\n"
    "s_add_u32 s52, s52, 14680064\n"
    "s_mov_b32 s53, 1049335798\n"
    "v_bfe_u32 v224, v1, 4, 1\n"
    "v_lshl_add_u32 v226, v224, 1, v3\n"
    "v_lshlrev_b32 v226, 9, v226\n"
    "v_and_b32 v224, 15, v1\n"
    "v_lshl_add_u32 v226, v224, 4, v226\n"
    "s_setprio 3\n"
    "v_mul_f32 v16, s53, v16\n"
    "v_mul_f32 v17, s53, v17\n"
    "v_mul_f32 v18, s53, v18\n"
    "v_mul_f32 v19, s53, v19\n"
    "v_mul_f32 v20, s53, v20\n"
    "v_mul_f32 v21, s53, v21\n"
    "v_mul_f32 v22, s53, v22\n"
    "v_mul_f32 v23, s53, v23\n"
    "v_mul_f32 v24, s53, v24\n"
    "v_mul_f32 v25, s53, v25\n"
    "v_mul_f32 v26, s53, v26\n"
    "v_mul_f32 v27, s53, v27\n"
    "v_mul_f32 v28, s53, v28\n"
    "v_mul_f32 v29, s53, v29\n"
    "v_mul_f32 v30, s53, v30\n"
    "v_mul_f32 v31, s53, v31\n"
    "v_mul_f32 v240, s53, v240\n"
    "v_mul_f32 v241, s53, v241\n"
    "v_mul_f32 v242, s53, v242\n"
    "v_mul_f32 v243, s53, v243\n"
    "v_mul_f32 v244, s53, v244\n"
    "v_mul_f32 v245, s53, v245\n"
    "v_mul_f32 v246, s53, v246\n"
    "v_mul_f32 v247, s53, v247\n"
    "v_mul_f32 v248, s53, v248\n"
    "v_mul_f32 v249, s53, v249\n"
    "v_mul_f32 v250, s53, v250\n"
    "v_mul_f32 v251, s53, v251\n"
    "v_mul_f32 v252, s53, v252\n"
    "v_mul_f32 v253, s53, v253\n"
    "v_mul_f32 v254, s53, v254\n"
    "v_mul_f32 v255, s53, v255\n"
    "v_cvt_pk_bf16_f32 v176, v16, v17\n"
    "v_cvt_pk_bf16_f32 v177, v18, v19\n"
    "v_cvt_pk_bf16_f32 v178, v20, v21\n"
    "v_cvt_pk_bf16_f32 v179, v22, v23\n"
    "s_nop 0\n"
    "v_permlane16_swap_b32 v176, v178\n"
    "v_permlane16_swap_b32 v177, v179\n"
    "buffer_store_dwordx4 v[176:179], v226, s[16:19], s52 offen offset:0 sc1\n"
    "v_cvt_pk_bf16_f32 v180, v24, v25\n"
    "v_cvt_pk_bf16_f32 v181, v26, v27\n"
    "v_cvt_pk_bf16_f32 v182, v28, v29\n"
    "v_cvt_pk_bf16_f32 v183, v30, v31\n"
    "s_nop 0\n"
    "v_permlane16_swap_b32 v180, v182\n"
    "v_permlane16_swap_b32 v181, v183\n"
    "buffer_store_dwordx4 v[180:183], v226, s[16:19], s52 offen offset:256 sc1\n"
    "v_cvt_pk_bf16_f32 v184, v240, v241\n"
    "v_cvt_pk_bf16_f32 v185, v242, v243\n"
    "v_cvt_pk_bf16_f32 v186, v244, v245\n"
    "v_cvt_pk_bf16_f32 v187, v246, v247\n"
    "s_nop 0\n"
    "v_permlane16_swap_b32 v184, v186\n"
    "v_permlane16_swap_b32 v185, v187\n"
    "buffer_store_dwordx4 v[184:187], v226, s[16:19], s52 offen offset:2048 sc1\n"
    "v_cvt_pk_bf16_f32 v188, v248, v249\n"
    "v_cvt_pk_bf16_f32 v189, v250, v251\n"
    "v_cvt_pk_bf16_f32 v190, v252, v253\n"
    "v_cvt_pk_bf16_f32 v191, v254, v255\n"
    "s_nop 0\n"
    "v_permlane16_swap_b32 v188, v190\n"
    "v_permlane16_swap_b32 v189, v191\n"
    "buffer_store_dwordx4 v[188:191], v226, s[16:19], s52 offen offset:2304 sc1\n"
    "s_setprio 2\n"
    "s_lshl_b32 s1, s66, 6\n"
    "s_add_u32 s1, s1, s62\n"
    "s_lshl_b32 s1, s1, 5\n"
    "s_add_u32 s1, s1, 393216\n"
    "s_add_u32 s52, s71, 36\n"
    "s_mul_i32 s71, s62, 56\n"
    "s_add_u32 s71, s71, s52\n"
    "s_lshl_b32 s71, s71, 11\n"
    "s_add_u32 s71, s71, 7340032\n"
    "v_mov_b32 v228, 163344\n"
    "s_mov_b32 s63, 0x100000\n"
    ".Lwp_1:\n"
    "ds_read_b128 v[224:227], v228\n"
    "s_waitcnt lgkmcnt(0)\n"
    "v_min3_u32 v229, v224, v225, v226\n"
    "v_min_u32 v229, v229, v227\n"
    "v_cmp_gt_u32 vcc, 2, v229\n"
    "s_cbranch_vccz .Lwpd_2\n"
    "s_sub_u32 s63, s63, 1\n"
    "s_cbranch_scc0 .Lwp_1\n"
    ".Lwpd_2:\n"
    "s_lshl2_add_u32 s52, s2, s47\n"
    "s_mul_i32 s53, s52, 1600\n"
    "s_mul_i32 s52, s52, 448\n"
    "v_lshlrev_b32 v225, 6, v1\n"
    "s_mov_b64 exec, 0x1ffffff\n"
    "buffer_load_dword v208, v225, s[28:31], s53 offen\n"
    "s_mov_b64 exec, 0x7f\n"
    "buffer_load_dword v209, v225, s[32:35], s52 offen\n"
    "s_mov_b64 exec, -1\n"
    "s_waitcnt vmcnt(2) lgkmcnt(0)\n"
    "v_mov_b64 v[224:225], s[48:49]\n"
    "s_mov_b64 exec, 1\n"
    "buffer_store_dwordx2 v[224:225], off, s[16:19], s1 sc1\n"
    "s_mov_b64 exec, -1\n"
    "s_cmp_eq_u32 s68, 3\n"
    "s_cbranch_scc1 .Lf_spec\n"
    "s_mov_b32 s75, 2\n"
    ".Lf_post:\n"
    "v_mov_b32 v212, v210\n"
    "v_xor_b32 v12, 98368, v6\n"
    "v_xor_b32 v13, 32, v12\n"
    "s_setprio 2\n"
    "s_call_b64 s[58:59], .Lfgemm\n"
    "s_setprio 2\n"
    "s_lshl_b32 s52, s45, 1\n"
    "s_mul_i32 s70, s62, 56\n"
    "s_add_u32 s70, s70, s52\n"
    "s_lshl_b32 s70, s70, 11\n"
    "s_add_u32 s70, s70, 7340032\n"
    "s_lshl4_add_u32 s3, s62, s45\n"
    "s_lshl_b32 s3, s3, 5\n"
    "s_add_u32 s3, s3, 475136\n"
    "s_lshl_b32 s53, s47, 11\n"
    "s_add_u32 s53, s53, 90112\n"
    "v_add_u32 v5, s53, v2\n"
    "s_cmp_lg_u32 s67, 0\n"
    "s_cbranch_scc1 .Lf_m2\n"
    "v_mov_b32 v80, v176\n"
    "v_mov_b32 v81, v177\n"
    "v_mov_b32 v82, v178\n"
    "v_mov_b32 v83, v179\n"
    "v_mov_b32 v84, v180\n"
    "v_mov_b32 v85, v181\n"
    "v_mov_b32 v86, v182\n"
    "v_mov_b32 v87, v183\n"
    "v_pk_mul_f32 v[192:193], v[240:241], s[0:1] op_sel_hi:[1,0]\n"
    "v_pk_mul_f32 v[194:195], v[242:243], s[0:1] op_sel_hi:[1,0]\n"
    "v_pk_mul_f32 v[196:197], v[244:245], s[0:1] op_sel_hi:[1,0]\n"
    "v_pk_mul_f32 v[198:199], v[246:247], s[0:1] op_sel_hi:[1,0]\n"
    "v_exp_f32 v192, v192\n"
    "v_exp_f32 v193, v193\n"
    "v_exp_f32 v194, v194\n"
    "v_exp_f32 v195, v195\n"
    "v_exp_f32 v196, v196\n"
    "v_exp_f32 v197, v197\n"
    "v_exp_f32 v198, v198\n"
    "v_exp_f32 v199, v199\n"
    "v_pk_add_f32 v[192:193], v[192:193], 1.0 op_sel_hi:[1,0]\n"
    "v_pk_add_f32 v[194:195], v[194:195], 1.0 op_sel_hi:[1,0]\n"
    "v_pk_add_f32 v[196:197], v[196:197], 1.0 op_sel_hi:[1,0]\n"
    "v_pk_add_f32 v[198:199], v[198:199], 1.0 op_sel_hi:[1,0]\n"
    "v_pk_fma_f32 v[192:193], v[192:193], v[192:193], 1.0 op_sel_hi:[1,1,0]\n"
    "v_pk_fma_f32 v[194:195], v[194:195], v[194:195], 1.0 op_sel_hi:[1,1,0]\n"
    "v_pk_fma_f32 v[196:197], v[196:197], v[196:197], 1.0 op_sel_hi:[1,1,0]\n"
    "v_pk_fma_f32 v[198:199], v[198:199], v[198:199], 1.0 op_sel_hi:[1,1,0]\n"
    "v_rcp_f32 v192, v192\n"
    "v_rcp_f32 v193, v193\n"
    "v_rcp_f32 v194, v194\n"
    "v_rcp_f32 v195, v195\n"
    "v_rcp_f32 v196, v196\n"
    "v_rcp_f32 v197, v197\n"
    "v_rcp_f32 v198, v198\n"
    "v_rcp_f32 v199, v199\n"
    "v_pk_fma_f32 v[192:193], v[192:193], -2.0, 1.0 op_sel_hi:[1,0,0]\n"
    "v_pk_fma_f32 v[194:195], v[194:195], -2.0, 1.0 op_sel_hi:[1,0,0]\n"
    "v_pk_fma_f32 v[196:197], v[196:197], -2.0, 1.0 op_sel_hi:[1,0,0]\n"
    "v_pk_fma_f32 v[198:199], v[198:199], -2.0, 1.0 op_sel_hi:[1,0,0]\n"
    "v_pk_mul_f32 v[192:193], v[192:193], v[240:241]\n"
    "v_pk_mul_f32 v[194:195], v[194:195], v[242:243]\n"
    "v_pk_mul_f32 v[196:197], v[196:197], v[244:245]\n"
    "v_pk_mul_f32 v[198:199], v[198:199], v[246:247]\n"
    "v_cvt_pk_bf16_f32 v88, v192, v193\n"
    "v_cvt_pk_bf16_f32 v89, v194, v195\n"
    "v_cvt_pk_bf16_f32 v90, v196, v197\n"
    "v_cvt_pk_bf16_f32 v91, v198, v199\n"
    "v_pk_mul_f32 v[200:201], v[248:249], s[0:1] op_sel_hi:[1,0]\n"
    "v_pk_mul_f32 v[202:203], v[250:251], s[0:1] op_sel_hi:[1,0]\n"
    "v_pk_mul_f32 v[204:205], v[252:253], s[0:1] op_sel_hi:[1,0]\n"
    "v_pk_mul_f32 v[206:207], v[254:255], s[0:1] op_sel_hi:[1,0]\n"
    "v_exp_f32 v200, v200\n"
    "v_exp_f32 v201, v201\n"
    "v_exp_f32 v202, v202\n"
    "v_exp_f32 v203, v203\n"
    "v_exp_f32 v204, v204\n"
    "v_exp_f32 v205, v205\n"
    "v_exp_f32 v206, v206\n"
    "v_exp_f32 v207, v207\n"
    "v_pk_add_f32 v[200:201], v[200:201], 1.0 op_sel_hi:[1,0]\n"
    "v_pk_add_f32 v[202:203], v[202:203], 1.0 op_sel_hi:[1,0]\n"
    "v_pk_add_f32 v[204:205], v[204:205], 1.0 op_sel_hi:[1,0]\n"
    "v_pk_add_f32 v[206:207], v[206:207], 1.0 op_sel_hi:[1,0]\n"
    "v_pk_fma_f32 v[200:201], v[200:201], v[200:201], 1.0 op_sel_hi:[1,1,0]\n"
    "v_pk_fma_f32 v[202:203], v[202:203], v[202:203], 1.0 op_sel_hi:[1,1,0]\n"
    "v_pk_fma_f32 v[204:205], v[204:205], v[204:205], 1.0 op_sel_hi:[1,1,0]\n"
    "v_pk_fma_f32 v[206:207], v[206:207], v[206:207], 1.0 op_sel_hi:[1,1,0]\n"
    "v_rcp_f32 v200, v200\n"
    "v_rcp_f32 v201, v201\n"
    "v_rcp_f32 v202, v202\n"
    "v_rcp_f32 v203, v203\n"
    "v_rcp_f32 v204, v204\n"
    "v_rcp_f32 v205, v205\n"
    "v_rcp_f32 v206, v206\n"
    "v_rcp_f32 v207, v207\n"
    "v_pk_fma_f32 v[200:201], v[200:201], -2.0, 1.0 op_sel_hi:[1,0,0]\n"
    "v_pk_fma_f32 v[202:203], v[202:203], -2.0, 1.0 op_sel_hi:[1,0,0]\n"
    "v_pk_fma_f32 v[204:205], v[204:205], -2.0, 1.0 op_sel_hi:[1,0,0]\n"
    "v_pk_fma_f32 v[206:207], v[206:207], -2.0, 1.0 op_sel_hi:[1,0,0]\n"
    "v_pk_mul_f32 v[200:201], v[200:201], v[248:249]\n"
    "v_pk_mul_f32 v[202:203], v[202:203], v[250:251]\n"
    "v_pk_mul_f32 v[204:205], v[204:205], v[252:253]\n"
    "v_pk_mul_f32 v[206:207], v[206:207], v[254:255]\n"
    "v_cvt_pk_bf16_f32 v92, v200, v201\n"
    "v_cvt_pk_bf16_f32 v93, v202, v203\n"
    "v_cvt_pk_bf16_f32 v94, v204, v205\n"
    "v_cvt_pk_bf16_f32 v95, v206, v207\n"
    "s_branch .Lf_m3\n"
    ".Lf_m2:\n"
    "s_cmp_lg_u32 s45, 2\n"
    "s_cbranch_scc1 .Lf_m3\n"
    "v_and_b32 v12, 48, v1\n"
    "v_add_u32 v12, 155648, v12\n"
    "ds_read_b128 v[32:35], v12 offset:0\n"
    "ds_read_b128 v[36:39], v12 offset:64\n"
    "ds_read_b128 v[40:43], v12 offset:128\n"
    "ds_read_b128 v[44:47], v12 offset:192\n"
    "ds_read_b128 v[48:51], v12 offset:256\n"
    "ds_read_b128 v[52:55], v12 offset:320\n"
    "s_waitcnt lgkmcnt(4)\n"
    "v_mfma_f32_16x16x32_bf16 v[240:243], v[32:35], v[64:67], 0\n"
    "ds_read_b128 v[56:59], v12 offset:384\n"
    "v_mfma_f32_16x16x32_bf16 v[240:243], v[36:39], v[72:75], v[240:243]\n"
    "v_mfma_f32_16x16x32_bf16 v[244:247], v[32:35], v[68:71], 0\n"
    "ds_read_b128 v[60:63], v12 offset:448\n"
    "v_mfma_f32_16x16x32_bf16 v[244:247], v[36:39], v[76:79], v[244:247]\n"
    "s_waitcnt lgkmcnt(4)\n"
    "v_mfma_f32_16x16x32_bf16 v[244:247], v[40:43], v[84:87], v[244:247]\n"
    "ds_read_b128 v[32:35], v12 offset:512\n"
    "v_mfma_f32_16x16x32_bf16 v[244:247], v[44:47], v[92:95], v[244:247]\n"
    "v_mfma_f32_16x16x32_bf16 v[240:243], v[40:43], v[80:83], v[240:243]\n"
    "ds_read_b128 v[36:39], v12 offset:576\n"
    "v_mfma_f32_16x16x32_bf16 v[240:243], v[44:47], v[88:91], v[240:243]\n"
    "s_waitcnt lgkmcnt(4)\n"
    "v_mfma_f32_16x16x32_bf16 v[240:243], v[48:51], v[96:99], v[240:243]\n"
    "ds_read_b128 v[40:43], v12 offset:640\n"
    "v_mfma_f32_16x16x32_bf16 v[240:243], v[52:55], v[104:107], v[240:243]\n"
    "v_mfma_f32_16x16x32_bf16 v[244:247], v[48:51], v[100:103], v[244:247]\n"
    "ds_read_b128 v[44:47], v12 offset:704\n"
    "v_mfma_f32_16x16x32_bf16 v[244:247], v[52:55], v[108:111], v[244:247]\n"
    "s_waitcnt lgkmcnt(4)\n"
    "v_mfma_f32_16x16x32_bf16 v[244:247], v[56:59], v[116:119], v[244:247]\n"
    "ds_read_b128 v[48:51], v12 offset:768\n"
    "v_mfma_f32_16x16x32_bf16 v[244:247], v[60:63], v[124:127], v[244:247]\n"
    "v_mfma_f32_16x16x32_bf16 v[240:243], v[56:59], v[112:115], v[240:243]\n"
    "ds_read_b128 v[52:55], v12 offset:832\n"
    "v_mfma_f32_16x16x32_bf16 v[240:243], v[60:63], v[120:123], v[240:243]\n"
    "s_waitcnt lgkmcnt(4)\n"
    "v_mfma_f32_16x16x32_bf16 v[240:243], v[32:35], v[128:131], v[240:243]\n"
    "v_mfma_f32_16x16x32_bf16 v[240:243], v[36:39], v[136:139], v[240:243]\n"
    "v_mfma_f32_16x16x32_bf16 v[244:247], v[32:35], v[132:135], v[244:247]\n"
    "v_mfma_f32_16x16x32_bf16 v[244:247], v[36:39], v[140:143], v[244:247]\n"
    "s_waitcnt lgkmcnt(2)\n"
    "v_mfma_f32_16x16x32_bf16 v[244:247], v[40:43], v[148:151], v[244:247]\n"
    "v_mfma_f32_16x16x32_bf16 v[244:247], v[44:47], v[156:159], v[244:247]\n"
    "v_mfma_f32_16x16x32_bf16 v[240:243], v[40:43], v[144:147], v[240:243]\n"
    "v_mfma_f32_16x16x32_bf16 v[240:243], v[44:47], v[152:155], v[240:243]\n"
    "s_waitcnt lgkmcnt(0)\n"
    "v_mfma_f32_16x16x32_bf16 v[240:243], v[48:51], v[160:163], v[240:243]\n"
    "v_mfma_f32_16x16x32_bf16 v[240:243], v[52:55], v[168:171], v[240:243]\n"
    "v_mfma_f32_16x16x32_bf16 v[244:247], v[48:51], v[164:167], v[244:247]\n"
    "v_mfma_f32_16x16x32_bf16 v[244:247], v[52:55], v[172:175], v[244:247]\n"
    "v_cmp_gt_u32 vcc, 16, v7\n"
    "s_nop 6\n"
    "v_cndmask_b32 v224, v244, v240, vcc\n"
    "v_mul_f32 v224, s0, v224\n"
    "v_exp_f32 v224, v224\n"
    "s_nop 0\n"
    "v_add_f32 v224, 1.0, v224\n"
    "v_rcp_f32 v232, v224\n"
    ".Lf_m3:\n"
    "v_pk_mul_f32 v[192:193], v[16:17], s[0:1] op_sel_hi:[1,0]\n"
    "v_pk_mul_f32 v[194:195], v[18:19], s[0:1] op_sel_hi:[1,0]\n"
    "v_pk_mul_f32 v[196:197], v[20:21], s[0:1] op_sel_hi:[1,0]\n"
    "v_pk_mul_f32 v[198:199], v[22:23], s[0:1] op_sel_hi:[1,0]\n"
    "v_exp_f32 v192, v192\n"
    "v_exp_f32 v193, v193\n"
    "v_exp_f32 v194, v194\n"
    "v_exp_f32 v195, v195\n"
    "v_exp_f32 v196, v196\n"
    "v_exp_f32 v197, v197\n"
    "v_exp_f32 v198, v198\n"
    "v_exp_f32 v199, v199\n"
    "v_pk_add_f32 v[192:193], v[192:193], 1.0 op_sel_hi:[1,0]\n"
    "v_pk_add_f32 v[194:195], v[194:195], 1.0 op_sel_hi:[1,0]\n"
    "v_pk_add_f32 v[196:197], v[196:197], 1.0 op_sel_hi:[1,0]\n"
    "v_pk_add_f32 v[198:199], v[198:199], 1.0 op_sel_hi:[1,0]\n"
    "v_pk_fma_f32 v[192:193], v[192:193], v[192:193], 1.0 op_sel_hi:[1,1,0]\n"
    "v_pk_fma_f32 v[194:195], v[194:195], v[194:195], 1.0 op_sel_hi:[1,1,0]\n"
    "v_pk_fma_f32 v[196:197], v[196:197], v[196:197], 1.0 op_sel_hi:[1,1,0]\n"
    "v_pk_fma_f32 v[198:199], v[198:199], v[198:199], 1.0 op_sel_hi:[1,1,0]\n"
    "v_rcp_f32 v192, v192\n"
    "v_rcp_f32 v193, v193\n"
    "v_rcp_f32 v194, v194\n"
    "v_rcp_f32 v195, v195\n"
    "v_rcp_f32 v196, v196\n"
    "v_rcp_f32 v197, v197\n"
    "v_rcp_f32 v198, v198\n"
    "v_rcp_f32 v199, v199\n"
    "v_pk_fma_f32 v[192:193], v[192:193], -2.0, 1.0 op_sel_hi:[1,0,0]\n"
    "v_pk_fma_f32 v[194:195], v[194:195], -2.0, 1.0 op_sel_hi:[1,0,0]\n"
    "v_pk_fma_f32 v[196:197], v[196:197], -2.0, 1.0 op_sel_hi:[1,0,0]\n"
    "v_pk_fma_f32 v[198:199], v[198:199], -2.0, 1.0 op_sel_hi:[1,0,0]\n"
    "v_pk_mul_f32 v[192:193], v[192:193], v[16:17]\n"
    "v_pk_mul_f32 v[194:195], v[194:195], v[18:19]\n"
    "v_pk_mul_f32 v[196:197], v[196:197], v[20:21]\n"
    "v_pk_mul_f32 v[198:199], v[198:199], v[22:23]\n"
    "v_cvt_pk_bf16_f32 v72, v192, v193\n"
    "v_cvt_pk_bf16_f32 v73, v194, v195\n"
    "v_cvt_pk_bf16_f32 v74, v196, v197\n"
    "v_cvt_pk_bf16_f32 v75, v198, v199\n"
    "v_pk_mul_f32 v[200:201], v[24:25], s[0:1] op_sel_hi:[1,0]\n"
    "v_pk_mul_f32 v[202:203], v[26:27], s[0:1] op_sel_hi:[1,0]\n"
    "v_pk_mul_f32 v[204:205], v[28:29], s[0:1] op_sel_hi:[1,0]\n"
    "v_pk_mul_f32 v[206:207], v[30:31], s[0:1] op_sel_hi:[1,0]\n"
    "v_exp_f32 v200, v200\n"
    "v_exp_f32 v201, v201\n"
    "v_exp_f32 v202, v202\n"
    "v_exp_f32 v203, v203\n"
    "v_exp_f32 v204, v204\n"
    "v_exp_f32 v205, v205\n"
    "v_exp_f32 v206, v206\n"
    "v_exp_f32 v207, v207\n"
    "v_pk_add_f32 v[200:201], v[200:201], 1.0 op_sel_hi:[1,0]\n"
    "v_pk_add_f32 v[202:203], v[202:203], 1.0 op_sel_hi:[1,0]\n"
    "v_pk_add_f32 v[204:205], v[204:205], 1.0 op_sel_hi:[1,0]\n"
    "v_pk_add_f32 v[206:207], v[206:207], 1.0 op_sel_hi:[1,0]\n"
    "v_pk_fma_f32 v[200:201], v[200:201], v[200:201], 1.0 op_sel_hi:[1,1,0]\n"
    "v_pk_fma_f32 v[202:203], v[202:203], v[202:203], 1.0 op_sel_hi:[1,1,0]\n"
    "v_pk_fma_f32 v[204:205], v[204:205], v[204:205], 1.0 op_sel_hi:[1,1,0]\n"
    "v_pk_fma_f32 v[206:207], v[206:207], v[206:207], 1.0 op_sel_hi:[1,1,0]\n"
    "v_rcp_f32 v200, v200\n"
    "v_rcp_f32 v201, v201\n"
    "v_rcp_f32 v202, v202\n"
    "v_rcp_f32 v203, v203\n"
    "v_rcp_f32 v204, v204\n"
    "v_rcp_f32 v205, v205\n"
    "v_rcp_f32 v206, v206\n"
    "v_rcp_f32 v207, v207\n"
    "v_pk_fma_f32 v[200:201], v[200:201], -2.0, 1.0 op_sel_hi:[1,0,0]\n"
    "v_pk_fma_f32 v[202:203], v[202:203], -2.0, 1.0 op_sel_hi:[1,0,0]\n"
    "v_pk_fma_f32 v[204:205], v[204:205], -2.0, 1.0 op_sel_hi:[1,0,0]\n"
    "v_pk_fma_f32 v[206:207], v[206:207], -2.0, 1.0 op_sel_hi:[1,0,0]\n"
    "v_pk_mul_f32 v[200:201], v[200:201], v[24:25]\n"
    "v_pk_mul_f32 v[202:203], v[202:203], v[26:27]\n"
    "v_pk_mul_f32 v[204:205], v[204:205], v[28:29]\n"
    "v_pk_mul_f32 v[206:207], v[206:207], v[30:31]\n"
    "v_cvt_pk_bf16_f32 v76, v200, v201\n"
    "v_cvt_pk_bf16_f32 v77, v202, v203\n"
    "v_cvt_pk_bf16_f32 v78, v204, v205\n"
    "v_cvt_pk_bf16_f32 v79, v206, v207\n"
    "s_branch .Lf_part2\n"
    ".Lf_spec:\n"
    "v_mov_b32 v224, 2\n"
    "ds_write_b32 v0, v224\n"
    "s_mov_b32 s63, 0x100000\n"
    "v_mov_b32 v24, 163328\n"
    "ds_read_b128 v[16:19], v24 offset:0\n"
    "ds_read_b128 v[20:23], v24 offset:16\n"
    ".Lw2_3:\n"
    "s_waitcnt lgkmcnt(0)\n"
    "v_min3_u32 v16, v16, v17, v18\n"
    "v_min3_u32 v20, v20, v21, v22\n"
    "v_min_u32 v20, v20, v23\n"
    "v_add_u32 v20, -1, v20\n"
    "v_min3_i32 v16, v16, v19, v20\n"
    "v_cmp_gt_i32 vcc, 2, v16\n"
    "s_cbranch_vccz .Lw2d_5\n"
    "ds_read_b128 v[16:19], v24 offset:0\n"
    "ds_read_b128 v[20:23], v24 offset:16\n"
    "s_sub_u32 s63, s63, 1\n"
    "s_cbranch_scc0 .Lw2_3\n"
    "s_waitcnt lgkmcnt(0)\n"
    ".Lw2d_5:\n"
    "v_mov_b32 v212, v210\n"
    "v_xor_b32 v12, 98368, v6\n"
    "v_xor_b32 v13, 32, v12\n"
    "s_setprio 2\n"
    "ds_read_b64_tr_b16 v[32:33], v12 offset:0\n"
    "ds_read_b64_tr_b16 v[34:35], v12 offset:512\n"
    "ds_read_b64_tr_b16 v[36:37], v12 offset:4096\n"
    "ds_read_b64_tr_b16 v[38:39], v12 offset:4608\n"
    "ds_read_b64_tr_b16 v[40:41], v12 offset:8192\n"
    "ds_read_b64_tr_b16 v[42:43], v12 offset:8704\n"
    "ds_read_b64_tr_b16 v[44:45], v12 offset:12288\n"
    "ds_read_b64_tr_b16 v[46:47], v12 offset:12800\n"
    "ds_read_b64_tr_b16 v[48:49], v12 offset:16384\n"
    "ds_read_b64_tr_b16 v[50:51], v12 offset:16896\n"
    "ds_read_b64_tr_b16 v[52:53], v12 offset:20480\n"
    "ds_read_b64_tr_b16 v[54:55], v12 offset:20992\n"
    "v_mfma_f32_16x16x32_bf16 v[16:19], v[212:215], v[216:219], 0\n"
    "v_mfma_f32_16x16x32_bf16 v[20:23], v[212:215], v[220:223], 0\n"
    "v_mfma_f32_16x16x32_bf16 v[24:27], v[212:215], v[216:219], 0\n"
    "v_mfma_f32_16x16x32_bf16 v[28:31], v[212:215], v[220:223], 0\n"
    "s_waitcnt lgkmcnt(8)\n"
    "v_mfma_f32_16x16x32_bf16 v[16:19], v[32:35], v[64:67], v[16:19]\n"
    "ds_read_b64_tr_b16 v[56:57], v12 offset:24576\n"
    "v_mfma_f32_16x16x32_bf16 v[16:19], v[36:39], v[72:75], v[16:19]\n"
    "ds_read_b64_tr_b16 v[58:59], v12 offset:25088\n"
    "s_add_u32 m0, s55, 0\n"
    "s_mul_i32 s57, s65, 229376\n"
    "s_add_u32 s57, s57, 2304\n"
    "buffer_load_dwordx4 v230, s[20:23], s57 offen lds\n"
    "v_mfma_f32_16x16x32_bf16 v[24:27], v[32:35], v[68:71], v[24:27]\n"
    "ds_read_b64_tr_b16 v[60:61], v12 offset:28672\n"
    "v_mfma_f32_16x16x32_bf16 v[24:27], v[36:39], v[76:79], v[24:27]\n"
    "ds_read_b64_tr_b16 v[62:63], v12 offset:29184\n"
    "s_waitcnt lgkmcnt(8)\n"
    "v_mfma_f32_16x16x32_bf16 v[24:27], v[40:43], v[84:87], v[24:27]\n"
    "ds_read_b64_tr_b16 v[32:33], v12 offset:32768\n"
    "v_mfma_f32_16x16x32_bf16 v[24:27], v[44:47], v[92:95], v[24:27]\n"
    "ds_read_b64_tr_b16 v[34:35], v12 offset:33280\n"
    "buffer_load_dwordx4 v231, s[20:23], s57 offen offset:1024 lds\n"
    "s_add_u32 s65, s65, 1\n"
    "s_cmp_eq_u32 s65, 7\n"
    "s_cselect_b32 s65, 0, s65\n"
    "s_setprio 3\n"
    "v_mfma_f32_16x16x32_bf16 v[16:19], v[40:43], v[80:83], v[16:19]\n"
    "ds_read_b64_tr_b16 v[36:37], v12 offset:36864\n"
    "v_mfma_f32_16x16x32_bf16 v[16:19], v[44:47], v[88:91], v[16:19]\n"
    "ds_read_b64_tr_b16 v[38:39], v12 offset:37376\n"
    "s_waitcnt lgkmcnt(8)\n"
    "v_mfma_f32_16x16x32_bf16 v[16:19], v[48:51], v[96:99], v[16:19]\n"
    "ds_read_b64_tr_b16 v[40:41], v12 offset:40960\n"
    "v_mfma_f32_16x16x32_bf16 v[16:19], v[52:55], v[104:107], v[16:19]\n"
    "ds_read_b64_tr_b16 v[42:43], v12 offset:41472\n"
    "s_add_u32 m0, s55, 8192\n"
    "s_mul_i32 s57, s65, 229376\n"
    "s_add_u32 s57, s57, 2304\n"
    "buffer_load_dwordx4 v230, s[20:23], s57 offen lds\n"
    "v_mfma_f32_16x16x32_bf16 v[24:27], v[48:51], v[100:103], v[24:27]\n"
    "ds_read_b64_tr_b16 v[44:45], v12 offset:45056\n"
    "v_mfma_f32_16x16x32_bf16 v[24:27], v[52:55], v[108:111], v[24:27]\n"
    "ds_read_b64_tr_b16 v[46:47], v12 offset:45568\n"
    "s_waitcnt lgkmcnt(8)\n"
    "v_mfma_f32_16x16x32_bf16 v[24:27], v[56:59], v[116:119], v[24:27]\n"
    "ds_read_b64_tr_b16 v[48:49], v12 offset:49152\n"
    "v_mfma_f32_16x16x32_bf16 v[24:27], v[60:63], v[124:127], v[24:27]\n"
    "ds_read_b64_tr_b16 v[50:51], v12 offset:49664\n"
    "buffer_load_dwordx4 v231, s[20:23], s57 offen offset:1024 lds\n"
    "s_add_u32 s65, s65, 1\n"
    "s_cmp_eq_u32 s65, 7\n"
    "s_cselect_b32 s65, 0, s65\n"
    "v_mfma_f32_16x16x32_bf16 v[16:19], v[56:59], v[112:115], v[16:19]\n"
    "ds_read_b64_tr_b16 v[52:53], v12 offset:53248\n"
    "v_mfma_f32_16x16x32_bf16 v[16:19], v[60:63], v[120:123], v[16:19]\n"
    "ds_read_b64_tr_b16 v[54:55], v12 offset:53760\n"
    "s_waitcnt lgkmcnt(8)\n"
    "v_mfma_f32_16x16x32_bf16 v[16:19], v[32:35], v[128:131], v[16:19]\n"
    "ds_read_b64_tr_b16 v[56:57], v13 offset:0\n"
    "v_mfma_f32_16x16x32_bf16 v[16:19], v[36:39], v[136:139], v[16:19]\n"
    "ds_read_b64_tr_b16 v[58:59], v13 offset:512\n"
    "s_add_u32 m0, s55, 16384\n"
    "s_mul_i32 s57, s65, 229376\n"
    "s_add_u32 s57, s57, 2304\n"
    "buffer_load_dwordx4 v230, s[20:23], s57 offen lds\n"
    "v_mfma_f32_16x16x32_bf16 v[24:27], v[32:35], v[132:135], v[24:27]\n"
    "ds_read_b64_tr_b16 v[60:61], v13 offset:4096\n"
    "v_mfma_f32_16x16x32_bf16 v[24:27], v[36:39], v[140:143], v[24:27]\n"
    "ds_read_b64_tr_b16 v[62:63], v13 offset:4608\n"
    "s_waitcnt lgkmcnt(8)\n"
    "v_mfma_f32_16x16x32_bf16 v[24:27], v[40:43], v[148:151], v[24:27]\n"
    "ds_read_b64_tr_b16 v[32:33], v13 offset:8192\n"
    "v_mfma_f32_16x16x32_bf16 v[24:27], v[44:47], v[156:159], v[24:27]\n"
    "ds_read_b64_tr_b16 v[34:35], v13 offset:8704\n"
    "buffer_load_dwordx4 v231, s[20:23], s57 offen offset:1024 lds\n"
    "s_add_u32 s65, s65, 1\n"
    "s_cmp_eq_u32 s65, 7\n"
    "s_cselect_b32 s65, 0, s65\n"
    "v_mfma_f32_16x16x32_bf16 v[16:19], v[40:43], v[144:147], v[16:19]\n"
    "ds_read_b64_tr_b16 v[36:37], v13 offset:12288\n"
    "v_mfma_f32_16x16x32_bf16 v[16:19], v[44:47], v[152:155], v[16:19]\n"
    "ds_read_b64_tr_b16 v[38:39], v13 offset:12800\n"
    "s_waitcnt lgkmcnt(8)\n"
    "v_mfma_f32_16x16x32_bf16 v[16:19], v[48:51], v[160:163], v[16:19]\n"
    "ds_read_b64_tr_b16 v[40:41], v13 offset:16384\n"
    "v_mfma_f32_16x16x32_bf16 v[16:19], v[52:55], v[168:171], v[16:19]\n"
    "ds_read_b64_tr_b16 v[42:43], v13 offset:16896\n"
    "s_add_u32 m0, s55, 24576\n"
    "s_mul_i32 s57, s65, 229376\n"
    "s_add_u32 s57, s57, 2304\n"
    "buffer_load_dwordx4 v230, s[20:23], s57 offen lds\n"
    "v_mfma_f32_16x16x32_bf16 v[24:27], v[48:51], v[164:167], v[24:27]\n"
    "ds_read_b64_tr_b16 v[44:45], v13 offset:20480\n"
    "v_mfma_f32_16x16x32_bf16 v[24:27], v[52:55], v[172:175], v[24:27]\n"
    "ds_read_b64_tr_b16 v[46:47], v13 offset:20992\n"
    "s_waitcnt lgkmcnt(8)\n"
    "v_mfma_f32_16x16x32_bf16 v[28:31], v[56:59], v[68:71], v[28:31]\n"
    "ds_read_b64_tr_b16 v[48:49], v13 offset:24576\n"
    "v_mfma_f32_16x16x32_bf16 v[28:31], v[60:63], v[76:79], v[28:31]\n"
    "ds_read_b64_tr_b16 v[50:51], v13 offset:25088\n"
    "buffer_load_dwordx4 v231, s[20:23], s57 offen offset:1024 lds\n"
    "s_add_u32 s65, s65, 1\n"
    "s_cmp_eq_u32 s65, 7\n"
    "s_cselect_b32 s65, 0, s65\n"
    "v_mfma_f32_16x16x32_bf16 v[20:23], v[56:59], v[64:67], v[20:23]\n"
    "ds_read_b64_tr_b16 v[52:53], v13 offset:28672\n"
    "v_mfma_f32_16x16x32_bf16 v[20:23], v[60:63], v[72:75], v[20:23]\n"
    "ds_read_b64_tr_b16 v[54:55], v13 offset:29184\n"
    "s_waitcnt lgkmcnt(8)\n"
    "v_mfma_f32_16x16x32_bf16 v[20:23], v[32:35], v[80:83], v[20:23]\n"
    "ds_read_b64_tr_b16 v[56:57], v13 offset:32768\n"
    "v_mfma_f32_16x16x32_bf16 v[20:23], v[36:39], v[88:91], v[20:23]\n"
    "ds_read_b64_tr_b16 v[58:59], v13 offset:33280\n"
    "s_add_u32 m0, s55, 32768\n"
    "s_mul_i32 s57, s65, 229376\n"
    "s_add_u32 s57, s57, 2304\n"
    "buffer_load_dwordx4 v230, s[20:23], s57 offen lds\n"
    "v_mfma_f32_16x16x32_bf16 v[28:31], v[32:35], v[84:87], v[28:31]\n"
    "ds_read_b64_tr_b16 v[60:61], v13 offset:36864\n"
    "v_mfma_f32_16x16x32_bf16 v[28:31], v[36:39], v[92:95], v[28:31]\n"
    "ds_read_b64_tr_b16 v[62:63], v13 offset:37376\n"
    "s_waitcnt lgkmcnt(8)\n"
    "v_mfma_f32_16x16x32_bf16 v[28:31], v[40:43], v[100:103], v[28:31]\n"
    "ds_read_b64_tr_b16 v[32:33], v13 offset:40960\n"
    "v_mfma_f32_16x16x32_bf16 v[28:31], v[44:47], v[108:111], v[28:31]\n"
    "ds_read_b64_tr_b16 v[34:35], v13 offset:41472\n"
    "buffer_load_dwordx4 v231, s[20:23], s57 offen offset:1024 lds\n"
    "s_add_u32 s65, s65, 1\n"
    "s_cmp_eq_u32 s65, 7\n"
    "s_cselect_b32 s65, 0, s65\n"
    "v_mfma_f32_16x16x32_bf16 v[20:23], v[40:43], v[96:99], v[20:23]\n"
    "ds_read_b64_tr_b16 v[36:37], v13 offset:45056\n"
    "v_mfma_f32_16x16x32_bf16 v[20:23], v[44:47], v[104:107], v[20:23]\n"
    "ds_read_b64_tr_b16 v[38:39], v13 offset:45568\n"
    "s_waitcnt lgkmcnt(8)\n"
    "v_mfma_f32_16x16x32_bf16 v[20:23], v[48:51], v[112:115], v[20:23]\n"
    "ds_read_b64_tr_b16 v[40:41], v13 offset:49152\n"
    "v_mfma_f32_16x16x32_bf16 v[20:23], v[52:55], v[120:123], v[20:23]\n"
    "ds_read_b64_tr_b16 v[42:43], v13 offset:49664\n"
    "s_add_u32 m0, s55, 40960\n"
    "s_mul_i32 s57, s65, 229376\n"
    "s_add_u32 s57, s57, 2304\n"
    "buffer_load_dwordx4 v230, s[20:23], s57 offen lds\n"
    "v_mfma_f32_16x16x32_bf16 v[28:31], v[48:51], v[116:119], v[28:31]\n"
    "ds_read_b64_tr_b16 v[44:45], v13 offset:53248\n"
    "v_mfma_f32_16x16x32_bf16 v[28:31], v[52:55], v[124:127], v[28:31]\n"
    "ds_read_b64_tr_b16 v[46:47], v13 offset:53760\n"
    "s_waitcnt lgkmcnt(8)\n"
    "v_mfma_f32_16x16x32_bf16 v[28:31], v[56:59], v[132:135], v[28:31]\n"
    "v_mfma_f32_16x16x32_bf16 v[28:31], v[60:63], v[140:143], v[28:31]\n"
    "buffer_load_dwordx4 v231, s[20:23], s57 offen offset:1024 lds\n"
    "s_add_u32 s65, s65, 1\n"
    "s_cmp_eq_u32 s65, 7\n"
    "s_cselect_b32 s65, 0, s65\n"
    "v_mfma_f32_16x16x32_bf16 v[20:23], v[56:59], v[128:131], v[20:23]\n"
    "v_mfma_f32_16x16x32_bf16 v[20:23], v[60:63], v[136:139], v[20:23]\n"
    "s_waitcnt lgkmcnt(4)\n"
    "v_mfma_f32_16x16x32_bf16 v[20:23], v[32:35], v[144:147], v[20:23]\n"
    "v_mfma_f32_16x16x32_bf16 v[20:23], v[36:39], v[152:155], v[20:23]\n"
    "s_add_u32 m0, s55, 49152\n"
    "s_mul_i32 s57, s65, 229376\n"
    "s_add_u32 s57, s57, 2304\n"
    "buffer_load_dwordx4 v230, s[20:23], s57 offen lds\n"
    "v_mfma_f32_16x16x32_bf16 v[28:31], v[32:35], v[148:151], v[28:31]\n"
    "v_mfma_f32_16x16x32_bf16 v[28:31], v[36:39], v[156:159], v[28:31]\n"
    "s_waitcnt lgkmcnt(0)\n"
    "v_mfma_f32_16x16x32_bf16 v[28:31], v[40:43], v[164:167], v[28:31]\n"
    "v_mfma_f32_16x16x32_bf16 v[28:31], v[44:47], v[172:175], v[28:31]\n"
    "buffer_load_dwordx4 v231, s[20:23], s57 offen offset:1024 lds\n"
    "s_add_u32 s65, s65, 1\n"
    "s_cmp_eq_u32 s65, 7\n"
    "s_cselect_b32 s65, 0, s65\n"
    "v_mfma_f32_16x16x32_bf16 v[20:23], v[40:43], v[160:163], v[20:23]\n"
    "v_mfma_f32_16x16x32_bf16 v[20:23], v[44:47], v[168:171], v[20:23]\n"
    "s_setprio 1\n"
    "s_setprio 2\n"
    "s_lshl_b32 s52, s45, 1\n"
    "s_mul_i32 s70, s62, 56\n"
    "s_add_u32 s70, s70, s52\n"
    "s_lshl_b32 s70, s70, 11\n"
    "s_add_u32 s70, s70, 7340032\n"
    "s_lshl4_add_u32 s3, s62, s45\n"
    "s_lshl_b32 s3, s3, 5\n"
    "s_add_u32 s3, s3, 475136\n"
    "s_lshl_b32 s53, s47, 11\n"
    "s_add_u32 s53, s53, 90112\n"
    "v_add_u32 v5, s53, v2\n"
    "s_call_b64 s[60:61], .Lfmish\n"
    "v_mov_b32 v232, v176\n"
    "v_mov_b32 v233, v177\n"
    "v_mov_b32 v234, v178\n"
    "v_mov_b32 v235, v179\n"
    "v_mov_b32 v236, v180\n"
    "v_mov_b32 v237, v181\n"
    "v_mov_b32 v238, v182\n"
    "v_mov_b32 v239, v183\n"
    "s_waitcnt vmcnt(0)\n"
    "v_mov_b32 v224, 3\n"
    "ds_write_b32 v0, v224\n"
    "v_mov_b32 v228, 163328\n"
    "s_mov_b32 s63, 0x100000\n"
    ".Lwp_6:\n"
    "ds_read_b128 v[224:227], v228\n"
    "s_waitcnt lgkmcnt(0)\n"
    "v_min3_u32 v229, v224, v225, v226\n"
    "v_min_u32 v229, v229, v227\n"
    "v_cmp_gt_u32 vcc, 3, v229\n"
    "s_cbranch_vccz .Lwpd_7\n"
    "s_sub_u32 s63, s63, 1\n"
    "s_cbranch_scc0 .Lwp_6\n"
    ".Lwpd_7:\n"
    "s_lshr_b32 s52, s45, 3\n"
    "s_lshl_b32 s52, s52, 6\n"
    "s_add_u32 s52, s52, 0\n"
    "v_mov_b32 v212, v211\n"
    "v_xor_b32 v12, s52, v6\n"
    "v_xor_b32 v13, 32, v12\n"
    "s_setprio 2\n"
    "s_call_b64 s[58:59], .Lfgemm\n"
    "s_setprio 2\n"
    "s_call_b64 s[60:61], .Lfmish\n"
    "v_mov_b32 v80, v176\n"
    "v_mov_b32 v81, v177\n"
    "v_mov_b32 v82, v178\n"
    "v_mov_b32 v83, v179\n"
    "v_mov_b32 v84, v180\n"
    "v_mov_b32 v85, v181\n"
    "v_mov_b32 v86, v182\n"
    "v_mov_b32 v87, v183\n"
    "v_mov_b32 v72, v232\n"
    "v_mov_b32 v73, v233\n"
    "v_mov_b32 v74, v234\n"
    "v_mov_b32 v75, v235\n"
    "v_mov_b32 v76, v236\n"
    "v_mov_b32 v77, v237\n"
    "v_mov_b32 v78, v238\n"
    "v_mov_b32 v79, v239\n"
    "s_mov_b32 s75, 3\n"
    "s_mov_b32 s34, 0\n"
    "s_branch .Lf_part2\n"
    ".Lfgemm:\n"
    "ds_read_b64_tr_b16 v[32:33], v12 offset:0\n"
    "ds_read_b64_tr_b16 v[34:35], v12 offset:512\n"
    "ds_read_b64_tr_b16 v[36:37], v12 offset:4096\n"
    "ds_read_b64_tr_b16 v[38:39], v12 offset:4608\n"
    "ds_read_b64_tr_b16 v[40:41], v12 offset:8192\n"
    "ds_read_b64_tr_b16 v[42:43], v12 offset:8704\n"
    "ds_read_b64_tr_b16 v[44:45], v12 offset:12288\n"
    "ds_read_b64_tr_b16 v[46:47], v12 offset:12800\n"
    "ds_read_b64_tr_b16 v[48:49], v12 offset:16384\n"
    "ds_read_b64_tr_b16 v[50:51], v12 offset:16896\n"
    "ds_read_b64_tr_b16 v[52:53], v12 offset:20480\n"
    "ds_read_b64_tr_b16 v[54:55], v12 offset:20992\n"
    "v_mfma_f32_16x16x32_bf16 v[16:19], v[212:215], v[216:219], 0\n"
    "v_mfma_f32_16x16x32_bf16 v[20:23], v[212:215], v[220:223], 0\n"
    "v_mfma_f32_16x16x32_bf16 v[24:27], v[212:215], v[216:219], 0\n"
    "v_mfma_f32_16x16x32_bf16 v[28:31], v[212:215], v[220:223], 0\n"
    "s_waitcnt lgkmcnt(8)\n"
    "v_mfma_f32_16x16x32_bf16 v[16:19], v[32:35], v[64:67], v[16:19]\n"
    "ds_read_b64_tr_b16 v[56:57], v12 offset:24576\n"
    "v_mfma_f32_16x16x32_bf16 v[16:19], v[36:39], v[72:75], v[16:19]\n"
    "ds_read_b64_tr_b16 v[58:59], v12 offset:25088\n"
    "v_mfma_f32_16x16x32_bf16 v[24:27], v[32:35], v[68:71], v[24:27]\n"
    "ds_read_b64_tr_b16 v[60:61], v12 offset:28672\n"
    "v_mfma_f32_16x16x32_bf16 v[24:27], v[36:39], v[76:79], v[24:27]\n"
    "ds_read_b64_tr_b16 v[62:63], v12 offset:29184\n"
    "s_waitcnt lgkmcnt(8)\n"
    "v_mfma_f32_16x16x32_bf16 v[24:27], v[40:43], v[84:87], v[24:27]\n"
    "ds_read_b64_tr_b16 v[32:33], v12 offset:32768\n"
    "v_mfma_f32_16x16x32_bf16 v[24:27], v[44:47], v[92:95], v[24:27]\n"
    "ds_read_b64_tr_b16 v[34:35], v12 offset:33280\n"
    "s_setprio 3\n"
    "v_mfma_f32_16x16x32_bf16 v[16:19], v[40:43], v[80:83], v[16:19]\n"
    "ds_read_b64_tr_b16 v[36:37], v12 offset:36864\n"
    "v_mfma_f32_16x16x32_bf16 v[16:19], v[44:47], v[88:91], v[16:19]\n"
    "ds_read_b64_tr_b16 v[38:39], v12 offset:37376\n"
    "s_waitcnt lgkmcnt(8)\n"
    "v_mfma_f32_16x16x32_bf16 v[16:19], v[48:51], v[96:99], v[16:19]\n"
    "ds_read_b64_tr_b16 v[40:41], v12 offset:40960\n"
    "v_mfma_f32_16x16x32_bf16 v[16:19], v[52:55], v[104:107], v[16:19]\n"
    "ds_read_b64_tr_b16 v[42:43], v12 offset:41472\n"
    "v_mfma_f32_16x16x32_bf16 v[24:27], v[48:51], v[100:103], v[24:27]\n"
    "ds_read_b64_tr_b16 v[44:45], v12 offset:45056\n"
    "v_mfma_f32_16x16x32_bf16 v[24:27], v[52:55], v[108:111], v[24:27]\n"
    "ds_read_b64_tr_b16 v[46:47], v12 offset:45568\n"
    "s_waitcnt lgkmcnt(8)\n"
    "v_mfma_f32_16x16x32_bf16 v[24:27], v[56:59], v[116:119], v[24:27]\n"
    "ds_read_b64_tr_b16 v[48:49], v12 offset:49152\n"
    "v_mfma_f32_16x16x32_bf16 v[24:27], v[60:63], v[124:127], v[24:27]\n"
    "ds_read_b64_tr_b16 v[50:51], v12 offset:49664\n"
    "v_mfma_f32_16x16x32_bf16 v[16:19], v[56:59], v[112:115], v[16:19]\n"
    "ds_read_b64_tr_b16 v[52:53], v12 offset:53248\n"
    "v_mfma_f32_16x16x32_bf16 v[16:19], v[60:63], v[120:123], v[16:19]\n"
    "ds_read_b64_tr_b16 v[54:55], v12 offset:53760\n"
    "s_waitcnt lgkmcnt(8)\n"
    "v_mfma_f32_16x16x32_bf16 v[16:19], v[32:35], v[128:131], v[16:19]\n"
    "ds_read_b64_tr_b16 v[56:57], v13 offset:0\n"
    "v_mfma_f32_16x16x32_bf16 v[16:19], v[36:39], v[136:139], v[16:19]\n"
    "ds_read_b64_tr_b16 v[58:59], v13 offset:512\n"
    "v_mfma_f32_16x16x32_bf16 v[24:27], v[32:35], v[132:135], v[24:27]\n"
    "ds_read_b64_tr_b16 v[60:61], v13 offset:4096\n"
    "v_mfma_f32_16x16x32_bf16 v[24:27], v[36:39], v[140:143], v[24:27]\n"
    "ds_read_b64_tr_b16 v[62:63], v13 offset:4608\n"
    "s_waitcnt lgkmcnt(8)\n"
    "v_mfma_f32_16x16x32_bf16 v[24:27], v[40:43], v[148:151], v[24:27]\n"
    "ds_read_b64_tr_b16 v[32:33], v13 offset:8192\n"
    "v_mfma_f32_16x16x32_bf16 v[24:27], v[44:47], v[156:159], v[24:27]\n"
    "ds_read_b64_tr_b16 v[34:35], v13 offset:8704\n"
    "v_mfma_f32_16x16x32_bf16 v[16:19], v[40:43], v[144:147], v[16:19]\n"
    "ds_read_b64_tr_b16 v[36:37], v13 offset:12288\n"
    "v_mfma_f32_16x16x32_bf16 v[16:19], v[44:47], v[152:155], v[16:19]\n"
    "ds_read_b64_tr_b16 v[38:39], v13 offset:12800\n"
    "s_waitcnt lgkmcnt(8)\n"
    "v_mfma_f32_16x16x32_bf16 v[16:19], v[48:51], v[160:163], v[16:19]\n"
    "ds_read_b64_tr_b16 v[40:41], v13 offset:16384\n"
    "v_mfma_f32_16x16x32_bf16 v[16:19], v[52:55], v[168:171], v[16:19]\n"
    "ds_read_b64_tr_b16 v[42:43], v13 offset:16896\n"
    "v_mfma_f32_16x16x32_bf16 v[24:27], v[48:51], v[164:167], v[24:27]\n"
    "ds_read_b64_tr_b16 v[44:45], v13 offset:20480\n"
    "v_mfma_f32_16x16x32_bf16 v[24:27], v[52:55], v[172:175], v[24:27]\n"
    "ds_read_b64_tr_b16 v[46:47], v13 offset:20992\n"
    "s_waitcnt lgkmcnt(8)\n"
    "v_mfma_f32_16x16x32_bf16 v[28:31], v[56:59], v[68:71], v[28:31]\n"
    "ds_read_b64_tr_b16 v[48:49], v13 offset:24576\n"
    "v_mfma_f32_16x16x32_bf16 v[28:31], v[60:63], v[76:79], v[28:31]\n"
    "ds_read_b64_tr_b16 v[50:51], v13 offset:25088\n"
    "v_mfma_f32_16x16x32_bf16 v[20:23], v[56:59], v[64:67], v[20:23]\n"
    "ds_read_b64_tr_b16 v[52:53], v13 offset:28672\n"
    "v_mfma_f32_16x16x32_bf16 v[20:23], v[60:63], v[72:75], v[20:23]\n"
    "ds_read_b64_tr_b16 v[54:55], v13 offset:29184\n"
    "s_waitcnt lgkmcnt(8)\n"
    "v_mfma_f32_16x16x32_bf16 v[20:23], v[32:35], v[80:83], v[20:23]\n"
    "ds_read_b64_tr_b16 v[56:57], v13 offset:32768\n"
    "v_mfma_f32_16x16x32_bf16 v[20:23], v[36:39], v[88:91], v[20:23]\n"
    "ds_read_b64_tr_b16 v[58:59], v13 offset:33280\n"
    "v_mfma_f32_16x16x32_bf16 v[28:31], v[32:35], v[84:87], v[28:31]\n"
    "ds_read_b64_tr_b16 v[60:61], v13 offset:36864\n"
    "v_mfma_f32_16x16x32_bf16 v[28:31], v[36:39], v[92:95], v[28:31]\n"
    "ds_read_b64_tr_b16 v[62:63], v13 offset:37376\n"
    "s_waitcnt lgkmcnt(8)\n"
    "v_mfma_f32_16x16x32_bf16 v[28:31], v[40:43], v[100:103], v[28:31]\n"
    "ds_read_b64_tr_b16 v[32:33], v13 offset:40960\n"
    "v_mfma_f32_16x16x32_bf16 v[28:31], v[44:47], v[108:111], v[28:31]\n"
    "ds_read_b64_tr_b16 v[34:35], v13 offset:41472\n"
    "v_mfma_f32_16x16x32_bf16 v[20:23], v[40:43], v[96:99], v[20:23]\n"
    "ds_read_b64_tr_b16 v[36:37], v13 offset:45056\n"
    "v_mfma_f32_16x16x32_bf16 v[20:23], v[44:47], v[104:107], v[20:23]\n"
    "ds_read_b64_tr_b16 v[38:39], v13 offset:45568\n"
    "s_waitcnt lgkmcnt(8)\n"
    "v_mfma_f32_16x16x32_bf16 v[20:23], v[48:51], v[112:115], v[20:23]\n"
    "ds_read_b64_tr_b16 v[40:41], v13 offset:49152\n"
    "v_mfma_f32_16x16x32_bf16 v[20:23], v[52:55], v[120:123], v[20:23]\n"
    "ds_read_b64_tr_b16 v[42:43], v13 offset:49664\n"
    "v_mfma_f32_16x16x32_bf16 v[28:31], v[48:51], v[116:119], v[28:31]\n"
    "ds_read_b64_tr_b16 v[44:45], v13 offset:53248\n"
    "v_mfma_f32_16x16x32_bf16 v[28:31], v[52:55], v[124:127], v[28:31]\n"
    "ds_read_b64_tr_b16 v[46:47], v13 offset:53760\n"
    "s_waitcnt lgkmcnt(8)\n"
    "v_mfma_f32_16x16x32_bf16 v[28:31], v[56:59], v[132:135], v[28:31]\n"
    "v_mfma_f32_16x16x32_bf16 v[28:31], v[60:63], v[140:143], v[28:31]\n"
    "v_mfma_f32_16x16x32_bf16 v[20:23], v[56:59], v[128:131], v[20:23]\n"
    "v_mfma_f32_16x16x32_bf16 v[20:23], v[60:63], v[136:139], v[20:23]\n"
    "s_waitcnt lgkmcnt(4)\n"
    "v_mfma_f32_16x16x32_bf16 v[20:23], v[32:35], v[144:147], v[20:23]\n"
    "v_mfma_f32_16x16x32_bf16 v[20:23], v[36:39], v[152:155], v[20:23]\n"
    "v_mfma_f32_16x16x32_bf16 v[28:31], v[32:35], v[148:151], v[28:31]\n"
    "v_mfma_f32_16x16x32_bf16 v[28:31], v[36:39], v[156:159], v[28:31]\n"
    "s_waitcnt lgkmcnt(0)\n"
    "v_mfma_f32_16x16x32_bf16 v[28:31], v[40:43], v[164:167], v[28:31]\n"
    "v_mfma_f32_16x16x32_bf16 v[28:31], v[44:47], v[172:175], v[28:31]\n"
    "v_mfma_f32_16x16x32_bf16 v[20:23], v[40:43], v[160:163], v[20:23]\n"
    "v_mfma_f32_16x16x32_bf16 v[20:23], v[44:47], v[168:171], v[20:23]\n"
    "s_setprio 1\n"
    "s_setpc_b64 s[58:59]\n"
    ".Lfmish:\n"
    "v_mul_f32 v192, s0, v16\n"
    "v_mul_f32 v193, s0, v17\n"
    "v_mul_f32 v194, s0, v18\n"
    "v_mul_f32 v195, s0, v19\n"
    "v_mul_f32 v196, s0, v20\n"
    "v_mul_f32 v197, s0, v21\n"
    "v_mul_f32 v198, s0, v22\n"
    "v_mul_f32 v199, s0, v23\n"
    "v_exp_f32 v192, v192\n"
    "v_exp_f32 v193, v193\n"
    "v_exp_f32 v194, v194\n"
    "v_exp_f32 v195, v195\n"
    "v_exp_f32 v196, v196\n"
    "v_exp_f32 v197, v197\n"
    "v_exp_f32 v198, v198\n"
    "v_exp_f32 v199, v199\n"
    "v_add_f32 v192, 1.0, v192\n"
    "v_add_f32 v193, 1.0, v193\n"
    "v_add_f32 v194, 1.0, v194\n"
    "v_add_f32 v195, 1.0, v195\n"
    "v_add_f32 v196, 1.0, v196\n"
    "v_add_f32 v197, 1.0, v197\n"
    "v_add_f32 v198, 1.0, v198\n"
    "v_add_f32 v199, 1.0, v199\n"
    "v_fma_f32 v192, v192, v192, 1.0\n"
    "v_fma_f32 v193, v193, v193, 1.0\n"
    "v_fma_f32 v194, v194, v194, 1.0\n"
    "v_fma_f32 v195, v195, v195, 1.0\n"
    "v_fma_f32 v196, v196, v196, 1.0\n"
    "v_fma_f32 v197, v197, v197, 1.0\n"
    "v_fma_f32 v198, v198, v198, 1.0\n"
    "v_fma_f32 v199, v199, v199, 1.0\n"
    "v_rcp_f32 v192, v192\n"
    "v_rcp_f32 v193, v193\n"
    "v_rcp_f32 v194, v194\n"
    "v_rcp_f32 v195, v195\n"
    "v_rcp_f32 v196, v196\n"
    "v_rcp_f32 v197, v197\n"
    "v_rcp_f32 v198, v198\n"
    "v_rcp_f32 v199, v199\n"
    "v_fma_f32 v192, v192, -2.0, 1.0\n"
    "v_fma_f32 v193, v193, -2.0, 1.0\n"
    "v_fma_f32 v194, v194, -2.0, 1.0\n"
    "v_fma_f32 v195, v195, -2.0, 1.0\n"
    "v_fma_f32 v196, v196, -2.0, 1.0\n"
    "v_fma_f32 v197, v197, -2.0, 1.0\n"
    "v_fma_f32 v198, v198, -2.0, 1.0\n"
    "v_fma_f32 v199, v199, -2.0, 1.0\n"
    "v_mul_f32 v192, v192, v16\n"
    "v_mul_f32 v193, v193, v17\n"
    "v_mul_f32 v194, v194, v18\n"
    "v_mul_f32 v195, v195, v19\n"
    "v_mul_f32 v196, v196, v20\n"
    "v_mul_f32 v197, v197, v21\n"
    "v_mul_f32 v198, v198, v22\n"
    "v_mul_f32 v199, v199, v23\n"
    "v_cvt_pk_bf16_f32 v176, v192, v193\n"
    "v_cvt_pk_bf16_f32 v177, v194, v195\n"
    "v_cvt_pk_bf16_f32 v178, v196, v197\n"
    "v_cvt_pk_bf16_f32 v179, v198, v199\n"
    "v_mul_f32 v200, s0, v24\n"
    "v_mul_f32 v201, s0, v25\n"
    "v_mul_f32 v202, s0, v26\n"
    "v_mul_f32 v203, s0, v27\n"
    "v_mul_f32 v204, s0, v28\n"
    "v_mul_f32 v205, s0, v29\n"
    "v_mul_f32 v206, s0, v30\n"
    "v_mul_f32 v207, s0, v31\n"
    "v_exp_f32 v200, v200\n"
    "v_exp_f32 v201, v201\n"
    "v_exp_f32 v202, v202\n"
    "v_exp_f32 v203, v203\n"
    "v_exp_f32 v204, v204\n"
    "v_exp_f32 v205, v205\n"
    "v_exp_f32 v206, v206\n"
    "v_exp_f32 v207, v207\n"
    "v_add_f32 v200, 1.0, v200\n"
    "v_add_f32 v201, 1.0, v201\n"
    "v_add_f32 v202, 1.0, v202\n"
    "v_add_f32 v203, 1.0, v203\n"
    "v_add_f32 v204, 1.0, v204\n"
    "v_add_f32 v205, 1.0, v205\n"
    "v_add_f32 v206, 1.0, v206\n"
    "v_add_f32 v207, 1.0, v207\n"
    "v_fma_f32 v200, v200, v200, 1.0\n"
    "v_fma_f32 v201, v201, v201, 1.0\n"
    "v_fma_f32 v202, v202, v202, 1.0\n"
    "v_fma_f32 v203, v203, v203, 1.0\n"
    "v_fma_f32 v204, v204, v204, 1.0\n"
    "v_fma_f32 v205, v205, v205, 1.0\n"
    "v_fma_f32 v206, v206, v206, 1.0\n"
    "v_fma_f32 v207, v207, v207, 1.0\n"
    "v_rcp_f32 v200, v200\n"
    "v_rcp_f32 v201, v201\n"
    "v_rcp_f32 v202, v202\n"
    "v_rcp_f32 v203, v203\n"
    "v_rcp_f32 v204, v204\n"
    "v_rcp_f32 v205, v205\n"
    "v_rcp_f32 v206, v206\n"
    "v_rcp_f32 v207, v207\n"
    "v_fma_f32 v200, v200, -2.0, 1.0\n"
    "v_fma_f32 v201, v201, -2.0, 1.0\n"
    "v_fma_f32 v202, v202, -2.0, 1.0\n"
    "v_fma_f32 v203, v203, -2.0, 1.0\n"
    "v_fma_f32 v204, v204, -2.0, 1.0\n"
    "v_fma_f32 v205, v205, -2.0, 1.0\n"
    "v_fma_f32 v206, v206, -2.0, 1.0\n"
    "v_fma_f32 v207, v207, -2.0, 1.0\n"
    "v_mul_f32 v200, v200, v24\n"
    "v_mul_f32 v201, v201, v25\n"
    "v_mul_f32 v202, v202, v26\n"
    "v_mul_f32 v203, v203, v27\n"
    "v_mul_f32 v204, v204, v28\n"
    "v_mul_f32 v205, v205, v29\n"
    "v_mul_f32 v206, v206, v30\n"
    "v_mul_f32 v207, v207, v31\n"
    "v_cvt_pk_bf16_f32 v180, v200, v201\n"
    "v_cvt_pk_bf16_f32 v181, v202, v203\n"
    "v_cvt_pk_bf16_f32 v182, v204, v205\n"
    "v_cvt_pk_bf16_f32 v183, v206, v207\n"
    "s_setpc_b64 s[60:61]\n"
    ".La_pro:\n"
    "s_setprio 1\n"
    "s_lshl_b32 s52, s64, 3\n"
    "v_add_u32 v12, s52, v1\n"
    "v_add_u32 v13, 4294967240, v12\n"
    "v_min_u32 v12, v12, v13\n"
    "v_lshlrev_b32 v12, 4, v12\n"
    "s_mov_b32 m0, 155648\n"
    "s_lshl4_add_u32 s52, s44, s45\n"
    "s_cmp_lg_u32 s52, 66\n"
    "s_waitcnt lgkmcnt(0)\n"
    "s_cbranch_scc1 .La_nogw\n"
    "buffer_load_dwordx4 v12, s[12:15], 0 offen lds\n"
    ".La_nogw:\n"
    "buffer_load_dwordx2 v[10:11], off, s[16:19], 0 sc1\n"
    "v_lshlrev_b32 v2, 4, v1\n"
    "v_and_b32 v7, 31, v1\n"
    "v_lshrrev_b32 v3, 5, v1\n"
    "v_lshrrev_b32 v12, 4, v1\n"
    "v_bfe_u32 v13, v1, 2, 2\n"
    "v_lshl_add_u32 v6, v12, 3, v13\n"
    "v_and_b32 v12, 1, v12\n"
    "v_and_b32 v13, 2, v13\n"
    "v_or_b32 v12, v12, v13\n"
    "v_lshl_add_u32 v6, v6, 2, v12\n"
    "v_and_b32 v12, 3, v1\n"
    "v_lshl_add_u32 v6, v6, 2, v12\n"
    "v_lshlrev_b32 v6, 3, v6\n"
    "v_bfe_u32 v12, v1, 1, 3\n"
    "v_lshrrev_b32 v14, 4, v1\n"
    "v_xor_b32 v12, v12, v14\n"
    "s_lshl_b32 s57, s47, 12\n"
    "v_and_b32 v13, 15, v1\n"
    "v_lshl_add_u32 v13, v13, 7, s57\n"
    "v_lshl_add_u32 v8, v12, 4, v13\n"
    "v_xor_b32 v9, 64, v8\n"
    "s_lshl_b32 s57, s44, 2\n"
    "s_add_u32 s57, s57, 163328\n"
    "v_mov_b32 v0, s57\n"
    "v_mov_b32 v213, 0\n"
    "v_mov_b32 v217, 0\n"
    "v_mov_b32 v221, 0\n"
    "v_mov_b32 v214, 0\n"
    "v_mov_b32 v218, 0\n"
    "v_mov_b32 v222, 0\n"
    "v_mov_b32 v215, 0\n"
    "v_mov_b32 v219, 0\n"
    "v_mov_b32 v223, 0\n"
    "ds_write_b32 v0, v213\n"
    "ds_write_b32 v0, v213 offset:32\n"
    "v_mov_b32 v13, 0x3f80\n"
    "v_cmp_eq_u32 vcc, 0, v14\n"
    "s_nop 1\n"
    "v_cndmask_b32 v216, 0, v13, vcc\n"
    "v_cmp_eq_u32 vcc, 1, v14\n"
    "s_nop 1\n"
    "v_cndmask_b32 v220, 0, v13, vcc\n"
    "s_mov_b32 s0, 0x3fb8aa3b\n"
    "s_lshl2_add_u32 s62, s46, s47\n"
    "v_lshlrev_b32 v225, 1, v7\n"
    "v_xor_b32 v12, 49152, v6\n"
    "v_xor_b32 v13, 32, v12\n"
    "v_xor_b32 v14, 64, v12\n"
    "v_xor_b32 v15, 96, v12\n"
    "buffer_load_ushort v208, v225, s[24:27], s56 offen offset:0\n"
    "s_and_b32 s52, s46, 7\n"
    "s_lshl2_add_u32 s52, s52, s47\n"
    "s_mul_i32 s52, s52, 50176\n"
    "s_add_u32 s52, s52, s56\n"
    "v_lshrrev_b32 v226, 1, v1\n"
    "v_mul_u32_u24 v226, 3584, v226\n"
    "v_and_b32 v227, 1, v1\n"
    "v_lshl_add_u32 v226, v227, 6, v226\n"
    "s_getpc_b64 s[58:59]\n"
    "s_add_u32 s58, s58, 0xffffd7fc\n"
    "s_addc_u32 s59, s59, -1\n"
    "s_lshr_b32 s54, s2, 3\n"
    "s_lshl2_add_u32 s54, s54, s47\n"
    "s_mul_i32 s53, s54, 320\n"
    "v_lshl_add_u32 v228, v1, 6, s53\n"
    "v_min_u32 v228, 0x8aac, v228\n"
    "s_mov_b64 exec, 31\n"
    "global_load_dword v210, v228, s[58:59]\n"
    "s_mov_b32 s70, s52\n"
    "s_mul_i32 s71, s54, 14336\n"
    "s_mov_b64 exec, -1\n"
    "v_mov_b32 v238, v226\n"
    "s_mul_i32 s52, s2, 576\n"
    "s_add_u32 s52, s52, 393216\n"
    "v_lshlrev_b32 v227, 6, v1\n"
    "s_cmp_lg_u32 s44, 5\n"
    "s_cbranch_scc1 .La_nft\n"
    "s_bfm_b64 exec, 9, 0\n"
    "buffer_load_dword v209, v227, s[16:19], s52 offen sc1\n"
    "s_mov_b64 exec, -1\n"
    ".La_nft:\n"
    "s_mov_b64 exec, 0xfffffff\n"
    "buffer_load_dword v209, v238, s[20:23], s70 offen\n"
    "s_mov_b64 exec, 0xff\n"
    "buffer_load_dword v209, v238, s[20:23], s71 offen offset:2304\n"
    "s_mov_b64 exec, -1\n"
    "s_add_u32 s54, s69, 5\n"
    "s_lshl_b32 s1, s54, 6\n"
    "s_add_u32 s1, s1, s62\n"
    "s_lshl_b32 s1, s1, 5\n"
    "s_add_u32 s1, s1, 393216\n"
    "s_mul_i32 s52, s45, 7\n"
    "s_lshr_b32 s52, s52, 1\n"
    "v_bfe_u32 v224, v1, 4, 1\n"
    "v_mad_u32_u24 v224, v224, 3, s52\n"
    "v_lshrrev_b32 v224, 3, v224\n"
    "v_mad_u32_u24 v225, v3, 7, v224\n"
    "v_and_b32 v236, 15, v1\n"
    "v_lshl_add_u32 v236, v225, 6, v236\n"
    "v_lshlrev_b32 v236, 5, v236\n"
    "v_and_b32 v237, 3, v1\n"
    "v_lshl_add_u32 v237, v224, 6, v237\n"
    "v_lshlrev_b32 v237, 5, v237\n"
    "s_lshl_b32 s72, s47, 9\n"
    "s_add_u32 s72, s72, 407552\n"
    "s_lshl_b32 s73, s46, 7\n"
    "s_add_u32 s73, s73, 393216\n"
    "s_lshl_b32 s52, s52, 9\n"
    "v_add_u32 v4, s52, v2\n"
    "s_mul_i32 s52, s45, 2048\n"
    "s_add_u32 s52, s52, 26624\n"
    "v_add_u32 v5, s52, v2\n"
    "s_mul_i32 s53, s47, 983040\n"
    "s_add_u32 s53, s53, 1048576\n"
    "s_add_u32 s40, s16, s53\n"
    "s_addc_u32 s41, s17, 0\n"
    "s_mov_b32 s42, 983040\n"
    "s_mov_b32 s43, s19\n"
    "s_mov_b32 s3, 0\n"
    "v_bfe_u32 v224, v1, 4, 1\n"
    "v_lshl_add_u32 v229, v224, 1, v3\n"
    "v_lshlrev_b32 v229, 9, v229\n"
    "v_and_b32 v228, 15, v1\n"
    "s_cmp_ge_u32 s69, 9\n"
    "s_cbranch_scc1 .Lkv_v\n"
    "v_lshl_add_u32 v230, v228, 4, v229\n"
    "v_add_u32 v231, 256, v230\n"
    "v_add_u32 v232, 2048, v230\n"
    "v_add_u32 v233, 2304, v230\n"
    "s_mul_i32 s52, s62, 15\n"
    "s_add_u32 s52, s52, s69\n"
    "s_lshl_b32 s52, s52, 12\n"
    "s_add_u32 s52, s52, 1040384\n"
    "s_mul_i32 s53, s62, 7\n"
    "s_add_u32 s53, s53, s69\n"
    "s_lshl_b32 s53, s53, 12\n"
    "s_add_u32 s53, s53, 14700544\n"
    "s_cmp_lt_u32 s45, 2\n"
    "s_cselect_b32 s52, s53, s52\n"
    "s_mov_b32 s53, 1049335798\n"
    "s_waitcnt lgkmcnt(0)\n"
    "s_barrier\n"
    "ds_read_b128 v[64:67], v8 offset:0\n"
    "ds_read_b128 v[68:71], v8 offset:2048\n"
    "ds_read_b128 v[72:75], v9 offset:0\n"
    "ds_read_b128 v[76:79], v9 offset:2048\n"
    "ds_read_b64_tr_b16 v[32:33], v12 offset:0\n"
    "ds_read_b64_tr_b16 v[34:35], v12 offset:512\n"
    "ds_read_b64_tr_b16 v[36:37], v12 offset:4096\n"
    "ds_read_b64_tr_b16 v[38:39], v12 offset:4608\n"
    "ds_read_b64_tr_b16 v[40:41], v13 offset:0\n"
    "ds_read_b64_tr_b16 v[42:43], v13 offset:512\n"
    "ds_read_b64_tr_b16 v[44:45], v13 offset:4096\n"
    "ds_read_b64_tr_b16 v[46:47], v13 offset:4608\n"
    "ds_read_b64_tr_b16 v[48:49], v14 offset:0\n"
    "ds_read_b64_tr_b16 v[50:51], v14 offset:512\n"
    "ds_read_b64_tr_b16 v[52:53], v14 offset:4096\n"
    "ds_read_b64_tr_b16 v[54:55], v14 offset:4608\n"
    "ds_read_b64_tr_b16 v[56:57], v15 offset:0\n"
    "ds_read_b64_tr_b16 v[58:59], v15 offset:512\n"
    "ds_read_b64_tr_b16 v[60:61], v15 offset:4096\n"
    "s_waitcnt lgkmcnt(11)\n"
    "v_mfma_f32_16x16x32_bf16 v[16:19], v[32:35], v[64:67], 0\n"
    "ds_read_b64_tr_b16 v[62:63], v15 offset:4608\n"
    "v_mfma_f32_16x16x32_bf16 v[16:19], v[36:39], v[72:75], v[16:19]\n"
    "v_mfma_f32_16x16x32_bf16 v[24:27], v[32:35], v[68:71], 0\n"
    "v_mfma_f32_16x16x32_bf16 v[24:27], v[36:39], v[76:79], v[24:27]\n"
    "s_waitcnt lgkmcnt(0)\n"
    "s_barrier\n"
    "ds_read_b128 v[80:83], v8 offset:16384\n"
    "ds_read_b128 v[84:87], v8 offset:18432\n"
    "ds_read_b128 v[88:91], v9 offset:16384\n"
    "ds_read_b128 v[92:95], v9 offset:18432\n"
    "ds_read_b64_tr_b16 v[32:33], v12 offset:8192\n"
    "v_mfma_f32_16x16x32_bf16 v[20:23], v[40:43], v[64:67], 0\n"
    "v_mfma_f32_16x16x32_bf16 v[20:23], v[44:47], v[72:75], v[20:23]\n"
    "ds_read_b64_tr_b16 v[34:35], v12 offset:8704\n"
    "ds_read_b64_tr_b16 v[36:37], v12 offset:12288\n"
    "v_mfma_f32_16x16x32_bf16 v[28:31], v[40:43], v[68:71], 0\n"
    "ds_read_b64_tr_b16 v[38:39], v12 offset:12800\n"
    "v_mfma_f32_16x16x32_bf16 v[28:31], v[44:47], v[76:79], v[28:31]\n"
    "ds_read_b64_tr_b16 v[40:41], v13 offset:8192\n"
    "v_mfma_f32_16x16x32_bf16 v[240:243], v[48:51], v[64:67], 0\n"
    "ds_read_b64_tr_b16 v[42:43], v13 offset:8704\n"
    "v_mfma_f32_16x16x32_bf16 v[240:243], v[52:55], v[72:75], v[240:243]\n"
    "ds_read_b64_tr_b16 v[44:45], v13 offset:12288\n"
    "v_mfma_f32_16x16x32_bf16 v[248:251], v[48:51], v[68:71], 0\n"
    "ds_read_b64_tr_b16 v[46:47], v13 offset:12800\n"
    "v_mfma_f32_16x16x32_bf16 v[248:251], v[52:55], v[76:79], v[248:251]\n"
    "ds_read_b64_tr_b16 v[48:49], v14 offset:8192\n"
    "v_mfma_f32_16x16x32_bf16 v[244:247], v[56:59], v[64:67], 0\n"
    "ds_read_b64_tr_b16 v[50:51], v14 offset:8704\n"
    "v_mfma_f32_16x16x32_bf16 v[244:247], v[60:63], v[72:75], v[244:247]\n"
    "ds_read_b64_tr_b16 v[52:53], v14 offset:12288\n"
    "v_mfma_f32_16x16x32_bf16 v[252:255], v[56:59], v[68:71], 0\n"
    "ds_read_b64_tr_b16 v[54:55], v14 offset:12800\n"
    "v_mfma_f32_16x16x32_bf16 v[252:255], v[60:63], v[76:79], v[252:255]\n"
    "ds_read_b64_tr_b16 v[56:57], v15 offset:8192\n"
    "ds_read_b64_tr_b16 v[58:59], v15 offset:8704\n"
    "ds_read_b64_tr_b16 v[60:61], v15 offset:12288\n"
    "s_waitcnt lgkmcnt(11)\n"
    "v_mfma_f32_16x16x32_bf16 v[16:19], v[32:35], v[80:83], v[16:19]\n"
    "ds_read_b64_tr_b16 v[62:63], v15 offset:12800\n"
    "v_mfma_f32_16x16x32_bf16 v[16:19], v[36:39], v[88:91], v[16:19]\n"
    "v_mfma_f32_16x16x32_bf16 v[24:27], v[32:35], v[84:87], v[24:27]\n"
    "v_mfma_f32_16x16x32_bf16 v[24:27], v[36:39], v[92:95], v[24:27]\n"
    "s_waitcnt vmcnt(3)\n"
    "v_readfirstlane_b32 s48, v10\n"
    "v_readfirstlane_b32 s49, v11\n"
    "s_add_u32 s48, s48, 0x7f4a7c15\n"
    "s_addc_u32 s49, s49, 0x9e3779b9\n"
    "s_waitcnt lgkmcnt(0)\n"
    "s_barrier\n"
    "ds_read_b128 v[96:99], v8 offset:32768\n"
    "ds_read_b128 v[100:103], v8 offset:34816\n"
    "ds_read_b128 v[104:107], v9 offset:32768\n"
    "ds_read_b128 v[108:111], v9 offset:34816\n"
    "ds_read_b64_tr_b16 v[32:33], v12 offset:16384\n"
    "v_mfma_f32_16x16x32_bf16 v[20:23], v[40:43], v[80:83], v[20:23]\n"
    "v_mfma_f32_16x16x32_bf16 v[20:23], v[44:47], v[88:91], v[20:23]\n"
    "ds_read_b64_tr_b16 v[34:35], v12 offset:16896\n"
    "ds_read_b64_tr_b16 v[36:37], v12 offset:20480\n"
    "v_mfma_f32_16x16x32_bf16 v[28:31], v[40:43], v[84:87], v[28:31]\n"
    "ds_read_b64_tr_b16 v[38:39], v12 offset:20992\n"
    "v_mfma_f32_16x16x32_bf16 v[28:31], v[44:47], v[92:95], v[28:31]\n"
    "ds_read_b64_tr_b16 v[40:41], v13 offset:16384\n"
    "v_mfma_f32_16x16x32_bf16 v[240:243], v[48:51], v[80:83], v[240:243]\n"
    "ds_read_b64_tr_b16 v[42:43], v13 offset:16896\n"
    "v_mfma_f32_16x16x32_bf16 v[240:243], v[52:55], v[88:91], v[240:243]\n"
    "ds_read_b64_tr_b16 v[44:45], v13 offset:20480\n"
    "v_mfma_f32_16x16x32_bf16 v[248:251], v[48:51], v[84:87], v[248:251]\n"
    "ds_read_b64_tr_b16 v[46:47], v13 offset:20992\n"
    "v_mfma_f32_16x16x32_bf16 v[248:251], v[52:55], v[92:95], v[248:251]\n"
    "ds_read_b64_tr_b16 v[48:49], v14 offset:16384\n"
    "v_mfma_f32_16x16x32_bf16 v[244:247], v[56:59], v[80:83], v[244:247]\n"
    "ds_read_b64_tr_b16 v[50:51], v14 offset:16896\n"
    "v_mfma_f32_16x16x32_bf16 v[244:247], v[60:63], v[88:91], v[244:247]\n"
    "ds_read_b64_tr_b16 v[52:53], v14 offset:20480\n"
    "v_mfma_f32_16x16x32_bf16 v[252:255], v[56:59], v[84:87], v[252:255]\n"
    "ds_read_b64_tr_b16 v[54:55], v14 offset:20992\n"
    "v_mfma_f32_16x16x32_bf16 v[252:255], v[60:63], v[92:95], v[252:255]\n"
    "ds_read_b64_tr_b16 v[56:57], v15 offset:16384\n"
    "ds_read_b64_tr_b16 v[58:59], v15 offset:16896\n"
    "ds_read_b64_tr_b16 v[60:61], v15 offset:20480\n"
    "s_waitcnt lgkmcnt(11)\n"
    "v_mfma_f32_16x16x32_bf16 v[16:19], v[32:35], v[96:99], v[16:19]\n"
    "ds_read_b64_tr_b16 v[62:63], v15 offset:20992\n"
    "v_mfma_f32_16x16x32_bf16 v[16:19], v[36:39], v[104:107], v[16:19]\n"
    "v_mfma_f32_16x16x32_bf16 v[24:27], v[32:35], v[100:103], v[24:27]\n"
    "v_mfma_f32_16x16x32_bf16 v[24:27], v[36:39], v[108:111], v[24:27]\n"
    "s_waitcnt lgkmcnt(0)\n"
    "s_barrier\n"
    "ds_read_b128 v[112:115], v8 offset:0\n"
    "ds_read_b128 v[116:119], v8 offset:2048\n"
    "ds_read_b128 v[120:123], v9 offset:0\n"
    "ds_read_b128 v[124:127], v9 offset:2048\n"
    "ds_read_b64_tr_b16 v[32:33], v12 offset:0\n"
    "v_mfma_f32_16x16x32_bf16 v[20:23], v[40:43], v[96:99], v[20:23]\n"
    "v_mfma_f32_16x16x32_bf16 v[20:23], v[44:47], v[104:107], v[20:23]\n"
    "ds_read_b64_tr_b16 v[34:35], v12 offset:512\n"
    "ds_read_b64_tr_b16 v[36:37], v12 offset:4096\n"
    "v_mfma_f32_16x16x32_bf16 v[28:31], v[40:43], v[100:103], v[28:31]\n"
    "ds_read_b64_tr_b16 v[38:39], v12 offset:4608\n"
    "v_mfma_f32_16x16x32_bf16 v[28:31], v[44:47], v[108:111], v[28:31]\n"
    "ds_read_b64_tr_b16 v[40:41], v13 offset:0\n"
    "v_mfma_f32_16x16x32_bf16 v[240:243], v[48:51], v[96:99], v[240:243]\n"
    "ds_read_b64_tr_b16 v[42:43], v13 offset:512\n"
    "v_mfma_f32_16x16x32_bf16 v[240:243], v[52:55], v[104:107], v[240:243]\n"
    "ds_read_b64_tr_b16 v[44:45], v13 offset:4096\n"
    "v_mfma_f32_16x16x32_bf16 v[248:251], v[48:51], v[100:103], v[248:251]\n"
    "ds_read_b64_tr_b16 v[46:47], v13 offset:4608\n"
    "v_mfma_f32_16x16x32_bf16 v[248:251], v[52:55], v[108:111], v[248:251]\n"
    "ds_read_b64_tr_b16 v[48:49], v14 offset:0\n"
    "v_mfma_f32_16x16x32_bf16 v[244:247], v[56:59], v[96:99], v[244:247]\n"
    "ds_read_b64_tr_b16 v[50:51], v14 offset:512\n"
    "v_mfma_f32_16x16x32_bf16 v[244:247], v[60:63], v[104:107], v[244:247]\n"
    "ds_read_b64_tr_b16 v[52:53], v14 offset:4096\n"
    "v_mfma_f32_16x16x32_bf16 v[252:255], v[56:59], v[100:103], v[252:255]\n"
    "ds_read_b64_tr_b16 v[54:55], v14 offset:4608\n"
    "v_mfma_f32_16x16x32_bf16 v[252:255], v[60:63], v[108:111], v[252:255]\n"
    "ds_read_b64_tr_b16 v[56:57], v15 offset:0\n"
    "ds_read_b64_tr_b16 v[58:59], v15 offset:512\n"
    "ds_read_b64_tr_b16 v[60:61], v15 offset:4096\n"
    "s_waitcnt lgkmcnt(11)\n"
    "v_mfma_f32_16x16x32_bf16 v[16:19], v[32:35], v[112:115], v[16:19]\n"
    "ds_read_b64_tr_b16 v[62:63], v15 offset:4608\n"
    "v_mfma_f32_16x16x32_bf16 v[16:19], v[36:39], v[120:123], v[16:19]\n"
    "v_mfma_f32_16x16x32_bf16 v[24:27], v[32:35], v[116:119], v[24:27]\n"
    "v_mfma_f32_16x16x32_bf16 v[24:27], v[36:39], v[124:127], v[24:27]\n"
    "s_waitcnt lgkmcnt(0)\n"
    "s_barrier\n"
    "ds_read_b128 v[128:131], v8 offset:16384\n"
    "ds_read_b128 v[132:135], v8 offset:18432\n"
    "ds_read_b128 v[136:139], v9 offset:16384\n"
    "ds_read_b128 v[140:143], v9 offset:18432\n"
    "ds_read_b64_tr_b16 v[32:33], v12 offset:8192\n"
    "v_mfma_f32_16x16x32_bf16 v[20:23], v[40:43], v[112:115], v[20:23]\n"
    "v_mfma_f32_16x16x32_bf16 v[20:23], v[44:47], v[120:123], v[20:23]\n"
    "ds_read_b64_tr_b16 v[34:35], v12 offset:8704\n"
    "ds_read_b64_tr_b16 v[36:37], v12 offset:12288\n"
    "v_mfma_f32_16x16x32_bf16 v[28:31], v[40:43], v[116:119], v[28:31]\n"
    "ds_read_b64_tr_b16 v[38:39], v12 offset:12800\n"
    "v_mfma_f32_16x16x32_bf16 v[28:31], v[44:47], v[124:127], v[28:31]\n"
    "ds_read_b64_tr_b16 v[40:41], v13 offset:8192\n"
    "v_mfma_f32_16x16x32_bf16 v[240:243], v[48:51], v[112:115], v[240:243]\n"
    "ds_read_b64_tr_b16 v[42:43], v13 offset:8704\n"
    "v_mfma_f32_16x16x32_bf16 v[240:243], v[52:55], v[120:123], v[240:243]\n"
    "ds_read_b64_tr_b16 v[44:45], v13 offset:12288\n"
    "v_mfma_f32_16x16x32_bf16 v[248:251], v[48:51], v[116:119], v[248:251]\n"
    "ds_read_b64_tr_b16 v[46:47], v13 offset:12800\n"
    "v_mfma_f32_16x16x32_bf16 v[248:251], v[52:55], v[124:127], v[248:251]\n"
    "ds_read_b64_tr_b16 v[48:49], v14 offset:8192\n"
    "v_mfma_f32_16x16x32_bf16 v[244:247], v[56:59], v[112:115], v[244:247]\n"
    "ds_read_b64_tr_b16 v[50:51], v14 offset:8704\n"
    "v_mfma_f32_16x16x32_bf16 v[244:247], v[60:63], v[120:123], v[244:247]\n"
    "ds_read_b64_tr_b16 v[52:53], v14 offset:12288\n"
    "v_mfma_f32_16x16x32_bf16 v[252:255], v[56:59], v[116:119], v[252:255]\n"
    "ds_read_b64_tr_b16 v[54:55], v14 offset:12800\n"
    "v_mfma_f32_16x16x32_bf16 v[252:255], v[60:63], v[124:127], v[252:255]\n"
    "ds_read_b64_tr_b16 v[56:57], v15 offset:8192\n"
    "ds_read_b64_tr_b16 v[58:59], v15 offset:8704\n"
    "ds_read_b64_tr_b16 v[60:61], v15 offset:12288\n"
    "s_waitcnt lgkmcnt(11)\n"
    "v_mfma_f32_16x16x32_bf16 v[16:19], v[32:35], v[128:131], v[16:19]\n"
    "ds_read_b64_tr_b16 v[62:63], v15 offset:12800\n"
    "v_mfma_f32_16x16x32_bf16 v[16:19], v[36:39], v[136:139], v[16:19]\n"
    "v_mfma_f32_16x16x32_bf16 v[24:27], v[32:35], v[132:135], v[24:27]\n"
    "v_mfma_f32_16x16x32_bf16 v[24:27], v[36:39], v[140:143], v[24:27]\n"
    "s_waitcnt lgkmcnt(0)\n"
    "s_barrier\n"
    "ds_read_b128 v[144:147], v8 offset:32768\n"
    "ds_read_b128 v[148:151], v8 offset:34816\n"
    "ds_read_b128 v[152:155], v9 offset:32768\n"
    "ds_read_b128 v[156:159], v9 offset:34816\n"
    "ds_read_b64_tr_b16 v[32:33], v12 offset:16384\n"
    "v_mfma_f32_16x16x32_bf16 v[20:23], v[40:43], v[128:131], v[20:23]\n"
    "v_mfma_f32_16x16x32_bf16 v[20:23], v[44:47], v[136:139], v[20:23]\n"
    "ds_read_b64_tr_b16 v[34:35], v12 offset:16896\n"
    "ds_read_b64_tr_b16 v[36:37], v12 offset:20480\n"
    "v_mfma_f32_16x16x32_bf16 v[28:31], v[40:43], v[132:135], v[28:31]\n"
    "ds_read_b64_tr_b16 v[38:39], v12 offset:20992\n"
    "v_mfma_f32_16x16x32_bf16 v[28:31], v[44:47], v[140:143], v[28:31]\n"
    "ds_read_b64_tr_b16 v[40:41], v13 offset:16384\n"
    "v_mfma_f32_16x16x32_bf16 v[240:243], v[48:51], v[128:131], v[240:243]\n"
    "ds_read_b64_tr_b16 v[42:43], v13 offset:16896\n"
    "v_mfma_f32_16x16x32_bf16 v[240:243], v[52:55], v[136:139], v[240:243]\n"
    "ds_read_b64_tr_b16 v[44:45], v13 offset:20480\n"
    "v_mfma_f32_16x16x32_bf16 v[248:251], v[48:51], v[132:135], v[248:251]\n"
    "ds_read_b64_tr_b16 v[46:47], v13 offset:20992\n"
    "v_mfma_f32_16x16x32_bf16 v[248:251], v[52:55], v[140:143], v[248:251]\n"
    "ds_read_b64_tr_b16 v[48:49], v14 offset:16384\n"
    "v_mfma_f32_16x16x32_bf16 v[244:247], v[56:59], v[128:131], v[244:247]\n"
    "ds_read_b64_tr_b16 v[50:51], v14 offset:16896\n"
    "v_mfma_f32_16x16x32_bf16 v[244:247], v[60:63], v[136:139], v[244:247]\n"
    "ds_read_b64_tr_b16 v[52:53], v14 offset:20480\n"
    "v_mfma_f32_16x16x32_bf16 v[252:255], v[56:59], v[132:135], v[252:255]\n"
    "ds_read_b64_tr_b16 v[54:55], v14 offset:20992\n"
    "v_mfma_f32_16x16x32_bf16 v[252:255], v[60:63], v[140:143], v[252:255]\n"
    "ds_read_b64_tr_b16 v[56:57], v15 offset:16384\n"
    "ds_read_b64_tr_b16 v[58:59], v15 offset:16896\n"
    "ds_read_b64_tr_b16 v[60:61], v15 offset:20480\n"
    "s_waitcnt lgkmcnt(11)\n"
    "v_mfma_f32_16x16x32_bf16 v[16:19], v[32:35], v[144:147], v[16:19]\n"
    "ds_read_b64_tr_b16 v[62:63], v15 offset:20992\n"
    "v_mfma_f32_16x16x32_bf16 v[16:19], v[36:39], v[152:155], v[16:19]\n"
    "v_mfma_f32_16x16x32_bf16 v[24:27], v[32:35], v[148:151], v[24:27]\n"
    "v_mfma_f32_16x16x32_bf16 v[24:27], v[36:39], v[156:159], v[24:27]\n"
    "s_waitcnt lgkmcnt(0)\n"
    "s_barrier\n"
    "ds_read_b128 v[160:163], v8 offset:0\n"
    "ds_read_b128 v[164:167], v8 offset:2048\n"
    "ds_read_b128 v[168:171], v9 offset:0\n"
    "ds_read_b128 v[172:175], v9 offset:2048\n"
    "ds_read_b64_tr_b16 v[32:33], v12 offset:0\n"
    "v_mfma_f32_16x16x32_bf16 v[20:23], v[40:43], v[144:147], v[20:23]\n"
    "v_mfma_f32_16x16x32_bf16 v[20:23], v[44:47], v[152:155], v[20:23]\n"
    "ds_read_b64_tr_b16 v[34:35], v12 offset:512\n"
    "ds_read_b64_tr_b16 v[36:37], v12 offset:4096\n"
    "v_mfma_f32_16x16x32_bf16 v[28:31], v[40:43], v[148:151], v[28:31]\n"
    "ds_read_b64_tr_b16 v[38:39], v12 offset:4608\n"
    "v_mfma_f32_16x16x32_bf16 v[28:31], v[44:47], v[156:159], v[28:31]\n"
    "ds_read_b64_tr_b16 v[40:41], v13 offset:0\n"
    "v_mfma_f32_16x16x32_bf16 v[240:243], v[48:51], v[144:147], v[240:243]\n"
    "ds_read_b64_tr_b16 v[42:43], v13 offset:512\n"
    "v_mfma_f32_16x16x32_bf16 v[240:243], v[52:55], v[152:155], v[240:243]\n"
    "ds_read_b64_tr_b16 v[44:45], v13 offset:4096\n"
    "v_mfma_f32_16x16x32_bf16 v[248:251], v[48:51], v[148:151], v[248:251]\n"
    "ds_read_b64_tr_b16 v[46:47], v13 offset:4608\n"
    "v_mfma_f32_16x16x32_bf16 v[248:251], v[52:55], v[156:159], v[248:251]\n"
    "ds_read_b64_tr_b16 v[48:49], v14 offset:0\n"
    "v_mfma_f32_16x16x32_bf16 v[244:247], v[56:59], v[144:147], v[244:247]\n"
    "ds_read_b64_tr_b16 v[50:51], v14 offset:512\n"
    "v_mfma_f32_16x16x32_bf16 v[244:247], v[60:63], v[152:155], v[244:247]\n"
    "ds_read_b64_tr_b16 v[52:53], v14 offset:4096\n"
    "v_mfma_f32_16x16x32_bf16 v[252:255], v[56:59], v[148:151], v[252:255]\n"
    "ds_read_b64_tr_b16 v[54:55], v14 offset:4608\n"
    "v_mfma_f32_16x16x32_bf16 v[252:255], v[60:63], v[156:159], v[252:255]\n"
    "ds_read_b64_tr_b16 v[56:57], v15 offset:0\n"
    "ds_read_b64_tr_b16 v[58:59], v15 offset:512\n"
    "ds_read_b64_tr_b16 v[60:61], v15 offset:4096\n"
    "s_waitcnt lgkmcnt(11)\n"
    "v_mfma_f32_16x16x32_bf16 v[16:19], v[32:35], v[160:163], v[16:19]\n"
    "ds_read_b64_tr_b16 v[62:63], v15 offset:4608\n"
    "v_mfma_f32_16x16x32_bf16 v[16:19], v[36:39], v[168:171], v[16:19]\n"
    "v_mfma_f32_16x16x32_bf16 v[24:27], v[32:35], v[164:167], v[24:27]\n"
    "v_mfma_f32_16x16x32_bf16 v[24:27], v[36:39], v[172:175], v[24:27]\n"
    "s_setprio 3\n"
    "s_waitcnt vmcnt(0)\n"
    "s_waitcnt lgkmcnt(0)\n"
    "s_barrier\n"
    "v_mfma_f32_16x16x32_bf16 v[20:23], v[40:43], v[160:163], v[20:23]\n"
    "v_mfma_f32_16x16x32_bf16 v[20:23], v[44:47], v[168:171], v[20:23]\n"
    "v_mfma_f32_16x16x32_bf16 v[28:31], v[40:43], v[164:167], v[28:31]\n"
    "v_mfma_f32_16x16x32_bf16 v[28:31], v[44:47], v[172:175], v[28:31]\n"
    "v_mfma_f32_16x16x32_bf16 v[240:243], v[48:51], v[160:163], v[240:243]\n"
    "v_mfma_f32_16x16x32_bf16 v[240:243], v[52:55], v[168:171], v[240:243]\n"
    "v_mfma_f32_16x16x32_bf16 v[248:251], v[48:51], v[164:167], v[248:251]\n"
    "v_mfma_f32_16x16x32_bf16 v[248:251], v[52:55], v[172:175], v[248:251]\n"
    "v_mfma_f32_16x16x32_bf16 v[244:247], v[56:59], v[160:163], v[244:247]\n"
    "v_mfma_f32_16x16x32_bf16 v[244:247], v[60:63], v[168:171], v[244:247]\n"
    "v_mfma_f32_16x16x32_bf16 v[252:255], v[56:59], v[164:167], v[252:255]\n"
    "v_mfma_f32_16x16x32_bf16 v[252:255], v[60:63], v[172:175], v[252:255]\n"
    "s_cmp_ge_u32 s45, 2\n"
    "s_cbranch_scc1 .Lkv_done\n"
    "v_mul_f32 v16, s53, v16\n"
    "v_mul_f32 v17, s53, v17\n"
    "v_mul_f32 v18, s53, v18\n"
    "v_mul_f32 v19, s53, v19\n"
    "v_mul_f32 v20, s53, v20\n"
    "v_mul_f32 v21, s53, v21\n"
    "v_mul_f32 v22, s53, v22\n"
    "v_mul_f32 v23, s53, v23\n"
    "v_mul_f32 v24, s53, v24\n"
    "v_mul_f32 v25, s53, v25\n"
    "v_mul_f32 v26, s53, v26\n"
    "v_mul_f32 v27, s53, v27\n"
    "v_mul_f32 v28, s53, v28\n"
    "v_mul_f32 v29, s53, v29\n"
    "v_mul_f32 v30, s53, v30\n"
    "v_mul_f32 v31, s53, v31\n"
    "v_mul_f32 v240, s53, v240\n"
    "v_mul_f32 v241, s53, v241\n"
    "v_mul_f32 v242, s53, v242\n"
    "v_mul_f32 v243, s53, v243\n"
    "v_mul_f32 v244, s53, v244\n"
    "v_mul_f32 v245, s53, v245\n"
    "v_mul_f32 v246, s53, v246\n"
    "v_mul_f32 v247, s53, v247\n"
    "v_mul_f32 v248, s53, v248\n"
    "v_mul_f32 v249, s53, v249\n"
    "v_mul_f32 v250, s53, v250\n"
    "v_mul_f32 v251, s53, v251\n"
    "v_mul_f32 v252, s53, v252\n"
    "v_mul_f32 v253, s53, v253\n"
    "v_mul_f32 v254, s53, v254\n"
    "v_mul_f32 v255, s53, v255\n"
    "s_branch .Lkv_done\n"
    ".Lkv_v:\n"
    "s_lshl_b32 s52, s69, 6\n"
    "s_add_u32 s52, s52, 4294966720\n"
    "v_add3_u32 v224, v228, s52, 0\n"
    "v_mul_u32_u24 v225, 2341, v224\n"
    "v_lshrrev_b32 v225, 16, v225\n"
    "v_mul_u32_u24 v225, 100, v225\n"
    "v_add_u32 v224, v224, v225\n"
    "v_lshl_add_u32 v230, v224, 4, v229\n"
    "v_add3_u32 v224, v228, s52, 16\n"
    "v_mul_u32_u24 v225, 2341, v224\n"
    "v_lshrrev_b32 v225, 16, v225\n"
    "v_mul_u32_u24 v225, 100, v225\n"
    "v_add_u32 v224, v224, v225\n"
    "v_lshl_add_u32 v231, v224, 4, v229\n"
    "v_add3_u32 v224, v228, s52, 32\n"
    "v_mul_u32_u24 v225, 2341, v224\n"
    "v_lshrrev_b32 v225, 16, v225\n"
    "v_mul_u32_u24 v225, 100, v225\n"
    "v_add_u32 v224, v224, v225\n"
    "v_lshl_add_u32 v232, v224, 4, v229\n"
    "v_add3_u32 v224, v228, s52, 48\n"
    "v_mul_u32_u24 v225, 2341, v224\n"
    "v_lshrrev_b32 v225, 16, v225\n"
    "v_mul_u32_u24 v225, 100, v225\n"
    "v_add_u32 v224, v224, v225\n"
    "v_lshl_add_u32 v233, v224, 4, v229\n"
    "s_mul_i32 s53, s52, 2341\n"
    "s_lshr_b32 s53, s53, 16\n"
    "v_lshrrev_b32 v224, 4, v1\n"
    "v_add_u32 v224, s53, v224\n"
    "v_mul_u32_u24 v225, 28, v224\n"
    "s_add_u32 s52, s52, 40\n"
    "v_cmp_gt_u32 s[50:51], s52, v225\n"
    "v_bfe_u32 v225, v1, 2, 2\n"
    "v_lshl_add_u32 v224, v224, 2, v225\n"
    "v_and_b32 v225, 3, v1\n"
    "v_lshl_add_u32 v224, v224, 5, v225\n"
    "v_lshlrev_b32 v234, 4, v224\n"
    "v_cmp_eq_u32 vcc, 0, v225\n"
    "v_mov_b32 v224, 0x3f803f80\n"
    "s_nop 0\n"
    "v_cndmask_b32 v192, 0, v224, vcc\n"
    "v_mov_b32 v193, v192\n"
    "v_mov_b32 v194, v192\n"
    "v_mov_b32 v195, v192\n"
    "s_mul_i32 s52, s62, 61440\n"
    "s_add_u32 s52, s52, 1077248\n"
    "s_waitcnt lgkmcnt(0)\n"
    "s_barrier\n"
    "ds_read_b128 v[64:67], v8 offset:0\n"
    "ds_read_b128 v[68:71], v8 offset:2048\n"
    "ds_read_b128 v[72:75], v9 offset:0\n"
    "ds_read_b128 v[76:79], v9 offset:2048\n"
    "ds_read_b64_tr_b16 v[32:33], v12 offset:0\n"
    "ds_read_b64_tr_b16 v[34:35], v12 offset:512\n"
    "ds_read_b64_tr_b16 v[36:37], v12 offset:4096\n"
    "ds_read_b64_tr_b16 v[38:39], v12 offset:4608\n"
    "ds_read_b64_tr_b16 v[40:41], v13 offset:0\n"
    "ds_read_b64_tr_b16 v[42:43], v13 offset:512\n"
    "ds_read_b64_tr_b16 v[44:45], v13 offset:4096\n"
    "ds_read_b64_tr_b16 v[46:47], v13 offset:4608\n"
    "ds_read_b64_tr_b16 v[48:49], v14 offset:0\n"
    "ds_read_b64_tr_b16 v[50:51], v14 offset:512\n"
    "ds_read_b64_tr_b16 v[52:53], v14 offset:4096\n"
    "ds_read_b64_tr_b16 v[54:55], v14 offset:4608\n"
    "ds_read_b64_tr_b16 v[56:57], v15 offset:0\n"
    "ds_read_b64_tr_b16 v[58:59], v15 offset:512\n"
    "ds_read_b64_tr_b16 v[60:61], v15 offset:4096\n"
    "s_waitcnt lgkmcnt(11)\n"
    "v_mfma_f32_16x16x32_bf16 v[16:19], v[64:67], v[32:35], 0\n"
    "ds_read_b64_tr_b16 v[62:63], v15 offset:4608\n"
    "v_mfma_f32_16x16x32_bf16 v[16:19], v[72:75], v[36:39], v[16:19]\n"
    "v_mfma_f32_16x16x32_bf16 v[20:23], v[68:71], v[32:35], 0\n"
    "v_mfma_f32_16x16x32_bf16 v[20:23], v[76:79], v[36:39], v[20:23]\n"
    "s_waitcnt lgkmcnt(0)\n"
    "s_barrier\n"
    "ds_read_b128 v[80:83], v8 offset:16384\n"
    "ds_read_b128 v[84:87], v8 offset:18432\n"
    "ds_read_b128 v[88:91], v9 offset:16384\n"
    "ds_read_b128 v[92:95], v9 offset:18432\n"
    "ds_read_b64_tr_b16 v[32:33], v12 offset:8192\n"
    "v_mfma_f32_16x16x32_bf16 v[24:27], v[64:67], v[40:43], 0\n"
    "v_mfma_f32_16x16x32_bf16 v[24:27], v[72:75], v[44:47], v[24:27]\n"
    "ds_read_b64_tr_b16 v[34:35], v12 offset:8704\n"
    "ds_read_b64_tr_b16 v[36:37], v12 offset:12288\n"
    "v_mfma_f32_16x16x32_bf16 v[28:31], v[68:71], v[40:43], 0\n"
    "ds_read_b64_tr_b16 v[38:39], v12 offset:12800\n"
    "v_mfma_f32_16x16x32_bf16 v[28:31], v[76:79], v[44:47], v[28:31]\n"
    "ds_read_b64_tr_b16 v[40:41], v13 offset:8192\n"
    "v_mfma_f32_16x16x32_bf16 v[240:243], v[64:67], v[48:51], 0\n"
    "ds_read_b64_tr_b16 v[42:43], v13 offset:8704\n"
    "v_mfma_f32_16x16x32_bf16 v[240:243], v[72:75], v[52:55], v[240:243]\n"
    "ds_read_b64_tr_b16 v[44:45], v13 offset:12288\n"
    "v_mfma_f32_16x16x32_bf16 v[244:247], v[68:71], v[48:51], 0\n"
    "ds_read_b64_tr_b16 v[46:47], v13 offset:12800\n"
    "v_mfma_f32_16x16x32_bf16 v[244:247], v[76:79], v[52:55], v[244:247]\n"
    "ds_read_b64_tr_b16 v[48:49], v14 offset:8192\n"
    "v_mfma_f32_16x16x32_bf16 v[248:251], v[64:67], v[56:59], 0\n"
    "ds_read_b64_tr_b16 v[50:51], v14 offset:8704\n"
    "v_mfma_f32_16x16x32_bf16 v[248:251], v[72:75], v[60:63], v[248:251]\n"
    "ds_read_b64_tr_b16 v[52:53], v14 offset:12288\n"
    "v_mfma_f32_16x16x32_bf16 v[252:255], v[68:71], v[56:59], 0\n"
    "ds_read_b64_tr_b16 v[54:55], v14 offset:12800\n"
    "v_mfma_f32_16x16x32_bf16 v[252:255], v[76:79], v[60:63], v[252:255]\n"
    "ds_read_b64_tr_b16 v[56:57], v15 offset:8192\n"
    "ds_read_b64_tr_b16 v[58:59], v15 offset:8704\n"
    "ds_read_b64_tr_b16 v[60:61], v15 offset:12288\n"
    "s_waitcnt lgkmcnt(11)\n"
    "v_mfma_f32_16x16x32_bf16 v[16:19], v[80:83], v[32:35], v[16:19]\n"
    "ds_read_b64_tr_b16 v[62:63], v15 offset:12800\n"
    "v_mfma_f32_16x16x32_bf16 v[16:19], v[88:91], v[36:39], v[16:19]\n"
    "v_mfma_f32_16x16x32_bf16 v[20:23], v[84:87], v[32:35], v[20:23]\n"
    "v_mfma_f32_16x16x32_bf16 v[20:23], v[92:95], v[36:39], v[20:23]\n"
    "s_waitcnt vmcnt(3)\n"
    "v_readfirstlane_b32 s48, v10\n"
    "v_readfirstlane_b32 s49, v11\n"
    "s_add_u32 s48, s48, 0x7f4a7c15\n"
    "s_addc_u32 s49, s49, 0x9e3779b9\n"
    "s_waitcnt lgkmcnt(0)\n"
    "s_barrier\n"
    "ds_read_b128 v[96:99], v8 offset:32768\n"
    "ds_read_b128 v[100:103], v8 offset:34816\n"
    "ds_read_b128 v[104:107], v9 offset:32768\n"
    "ds_read_b128 v[108:111], v9 offset:34816\n"
    "ds_read_b64_tr_b16 v[32:33], v12 offset:16384\n"
    "v_mfma_f32_16x16x32_bf16 v[24:27], v[80:83], v[40:43], v[24:27]\n"
    "v_mfma_f32_16x16x32_bf16 v[24:27], v[88:91], v[44:47], v[24:27]\n"
    "ds_read_b64_tr_b16 v[34:35], v12 offset:16896\n"
    "ds_read_b64_tr_b16 v[36:37], v12 offset:20480\n"
    "v_mfma_f32_16x16x32_bf16 v[28:31], v[84:87], v[40:43], v[28:31]\n"
    "ds_read_b64_tr_b16 v[38:39], v12 offset:20992\n"
    "v_mfma_f32_16x16x32_bf16 v[28:31], v[92:95], v[44:47], v[28:31]\n"
    "ds_read_b64_tr_b16 v[40:41], v13 offset:16384\n"
    "v_mfma_f32_16x16x32_bf16 v[240:243], v[80:83], v[48:51], v[240:243]\n"
    "ds_read_b64_tr_b16 v[42:43], v13 offset:16896\n"
    "v_mfma_f32_16x16x32_bf16 v[240:243], v[88:91], v[52:55], v[240:243]\n"
    "ds_read_b64_tr_b16 v[44:45], v13 offset:20480\n"
    "v_mfma_f32_16x16x32_bf16 v[244:247], v[84:87], v[48:51], v[244:247]\n"
    "ds_read_b64_tr_b16 v[46:47], v13 offset:20992\n"
    "v_mfma_f32_16x16x32_bf16 v[244:247], v[92:95], v[52:55], v[244:247]\n"
    "ds_read_b64_tr_b16 v[48:49], v14 offset:16384\n"
    "v_mfma_f32_16x16x32_bf16 v[248:251], v[80:83], v[56:59], v[248:251]\n"
    "ds_read_b64_tr_b16 v[50:51], v14 offset:16896\n"
    "v_mfma_f32_16x16x32_bf16 v[248:251], v[88:91], v[60:63], v[248:251]\n"
    "ds_read_b64_tr_b16 v[52:53], v14 offset:20480\n"
    "v_mfma_f32_16x16x32_bf16 v[252:255], v[84:87], v[56:59], v[252:255]\n"
    "ds_read_b64_tr_b16 v[54:55], v14 offset:20992\n"
    "v_mfma_f32_16x16x32_bf16 v[252:255], v[92:95], v[60:63], v[252:255]\n"
    "ds_read_b64_tr_b16 v[56:57], v15 offset:16384\n"
    "ds_read_b64_tr_b16 v[58:59], v15 offset:16896\n"
    "ds_read_b64_tr_b16 v[60:61], v15 offset:20480\n"
    "s_waitcnt lgkmcnt(11)\n"
    "v_mfma_f32_16x16x32_bf16 v[16:19], v[96:99], v[32:35], v[16:19]\n"
    "ds_read_b64_tr_b16 v[62:63], v15 offset:20992\n"
    "v_mfma_f32_16x16x32_bf16 v[16:19], v[104:107], v[36:39], v[16:19]\n"
    "v_mfma_f32_16x16x32_bf16 v[20:23], v[100:103], v[32:35], v[20:23]\n"
    "v_mfma_f32_16x16x32_bf16 v[20:23], v[108:111], v[36:39], v[20:23]\n"
    "s_waitcnt lgkmcnt(0)\n"
    "s_barrier\n"
    "ds_read_b128 v[112:115], v8 offset:0\n"
    "ds_read_b128 v[116:119], v8 offset:2048\n"
    "ds_read_b128 v[120:123], v9 offset:0\n"
    "ds_read_b128 v[124:127], v9 offset:2048\n"
    "ds_read_b64_tr_b16 v[32:33], v12 offset:0\n"
    "v_mfma_f32_16x16x32_bf16 v[24:27], v[96:99], v[40:43], v[24:27]\n"
    "v_mfma_f32_16x16x32_bf16 v[24:27], v[104:107], v[44:47], v[24:27]\n"
    "ds_read_b64_tr_b16 v[34:35], v12 offset:512\n"
    "ds_read_b64_tr_b16 v[36:37], v12 offset:4096\n"
    "v_mfma_f32_16x16x32_bf16 v[28:31], v[100:103], v[40:43], v[28:31]\n"
    "ds_read_b64_tr_b16 v[38:39], v12 offset:4608\n"
    "v_mfma_f32_16x16x32_bf16 v[28:31], v[108:111], v[44:47], v[28:31]\n"
    "ds_read_b64_tr_b16 v[40:41], v13 offset:0\n"
    "v_mfma_f32_16x16x32_bf16 v[240:243], v[96:99], v[48:51], v[240:243]\n"
    "ds_read_b64_tr_b16 v[42:43], v13 offset:512\n"
    "v_mfma_f32_16x16x32_bf16 v[240:243], v[104:107], v[52:55], v[240:243]\n"
    "ds_read_b64_tr_b16 v[44:45], v13 offset:4096\n"
    "v_mfma_f32_16x16x32_bf16 v[244:247], v[100:103], v[48:51], v[244:247]\n"
    "ds_read_b64_tr_b16 v[46:47], v13 offset:4608\n"
    "v_mfma_f32_16x16x32_bf16 v[244:247], v[108:111], v[52:55], v[244:247]\n"
    "ds_read_b64_tr_b16 v[48:49], v14 offset:0\n"
    "v_mfma_f32_16x16x32_bf16 v[248:251], v[96:99], v[56:59], v[248:251]\n"
    "ds_read_b64_tr_b16 v[50:51], v14 offset:512\n"
    "v_mfma_f32_16x16x32_bf16 v[248:251], v[104:107], v[60:63], v[248:251]\n"
    "ds_read_b64_tr_b16 v[52:53], v14 offset:4096\n"
    "v_mfma_f32_16x16x32_bf16 v[252:255], v[100:103], v[56:59], v[252:255]\n"
    "ds_read_b64_tr_b16 v[54:55], v14 offset:4608\n"
    "v_mfma_f32_16x16x32_bf16 v[252:255], v[108:111], v[60:63], v[252:255]\n"
    "ds_read_b64_tr_b16 v[56:57], v15 offset:0\n"
    "ds_read_b64_tr_b16 v[58:59], v15 offset:512\n"
    "ds_read_b64_tr_b16 v[60:61], v15 offset:4096\n"
    "s_waitcnt lgkmcnt(11)\n"
    "v_mfma_f32_16x16x32_bf16 v[16:19], v[112:115], v[32:35], v[16:19]\n"
    "ds_read_b64_tr_b16 v[62:63], v15 offset:4608\n"
    "v_mfma_f32_16x16x32_bf16 v[16:19], v[120:123], v[36:39], v[16:19]\n"
    "v_mfma_f32_16x16x32_bf16 v[20:23], v[116:119], v[32:35], v[20:23]\n"
    "v_mfma_f32_16x16x32_bf16 v[20:23], v[124:127], v[36:39], v[20:23]\n"
    "s_waitcnt lgkmcnt(0)\n"
    "s_barrier\n"
    "ds_read_b128 v[128:131], v8 offset:16384\n"
    "ds_read_b128 v[132:135], v8 offset:18432\n"
    "ds_read_b128 v[136:139], v9 offset:16384\n"
    "ds_read_b128 v[140:143], v9 offset:18432\n"
    "ds_read_b64_tr_b16 v[32:33], v12 offset:8192\n"
    "v_mfma_f32_16x16x32_bf16 v[24:27], v[112:115], v[40:43], v[24:27]\n"
    "v_mfma_f32_16x16x32_bf16 v[24:27], v[120:123], v[44:47], v[24:27]\n"
    "ds_read_b64_tr_b16 v[34:35], v12 offset:8704\n"
    "ds_read_b64_tr_b16 v[36:37], v12 offset:12288\n"
    "v_mfma_f32_16x16x32_bf16 v[28:31], v[116:119], v[40:43], v[28:31]\n"
    "ds_read_b64_tr_b16 v[38:39], v12 offset:12800\n"
    "v_mfma_f32_16x16x32_bf16 v[28:31], v[124:127], v[44:47], v[28:31]\n"
    "ds_read_b64_tr_b16 v[40:41], v13 offset:8192\n"
    "v_mfma_f32_16x16x32_bf16 v[240:243], v[112:115], v[48:51], v[240:243]\n"
    "ds_read_b64_tr_b16 v[42:43], v13 offset:8704\n"
    "v_mfma_f32_16x16x32_bf16 v[240:243], v[120:123], v[52:55], v[240:243]\n"
    "ds_read_b64_tr_b16 v[44:45], v13 offset:12288\n"
    "v_mfma_f32_16x16x32_bf16 v[244:247], v[116:119], v[48:51], v[244:247]\n"
    "ds_read_b64_tr_b16 v[46:47], v13 offset:12800\n"
    "v_mfma_f32_16x16x32_bf16 v[244:247], v[124:127], v[52:55], v[244:247]\n"
    "ds_read_b64_tr_b16 v[48:49], v14 offset:8192\n"
    "v_mfma_f32_16x16x32_bf16 v[248:251], v[112:115], v[56:59], v[248:251]\n"
    "ds_read_b64_tr_b16 v[50:51], v14 offset:8704\n"
    "v_mfma_f32_16x16x32_bf16 v[248:251], v[120:123], v[60:63], v[248:251]\n"
    "ds_read_b64_tr_b16 v[52:53], v14 offset:12288\n"
    "v_mfma_f32_16x16x32_bf16 v[252:255], v[116:119], v[56:59], v[252:255]\n"
    "ds_read_b64_tr_b16 v[54:55], v14 offset:12800\n"
    "v_mfma_f32_16x16x32_bf16 v[252:255], v[124:127], v[60:63], v[252:255]\n"
    "ds_read_b64_tr_b16 v[56:57], v15 offset:8192\n"
    "ds_read_b64_tr_b16 v[58:59], v15 offset:8704\n"
    "ds_read_b64_tr_b16 v[60:61], v15 offset:12288\n"
    "s_waitcnt lgkmcnt(11)\n"
    "v_mfma_f32_16x16x32_bf16 v[16:19], v[128:131], v[32:35], v[16:19]\n"
    "ds_read_b64_tr_b16 v[62:63], v15 offset:12800\n"
    "v_mfma_f32_16x16x32_bf16 v[16:19], v[136:139], v[36:39], v[16:19]\n"
    "v_mfma_f32_16x16x32_bf16 v[20:23], v[132:135], v[32:35], v[20:23]\n"
    "v_mfma_f32_16x16x32_bf16 v[20:23], v[140:143], v[36:39], v[20:23]\n"
    "s_waitcnt lgkmcnt(0)\n"
    "s_barrier\n"
    "ds_read_b128 v[144:147], v8 offset:32768\n"
    "ds_read_b128 v[148:151], v8 offset:34816\n"
    "ds_read_b128 v[152:155], v9 offset:32768\n"
    "ds_read_b128 v[156:159], v9 offset:34816\n"
    "ds_read_b64_tr_b16 v[32:33], v12 offset:16384\n"
    "v_mfma_f32_16x16x32_bf16 v[24:27], v[128:131], v[40:43], v[24:27]\n"
    "v_mfma_f32_16x16x32_bf16 v[24:27], v[136:139], v[44:47], v[24:27]\n"
    "ds_read_b64_tr_b16 v[34:35], v12 offset:16896\n"
    "ds_read_b64_tr_b16 v[36:37], v12 offset:20480\n"
    "v_mfma_f32_16x16x32_bf16 v[28:31], v[132:135], v[40:43], v[28:31]\n"
    "ds_read_b64_tr_b16 v[38:39], v12 offset:20992\n"
    "v_mfma_f32_16x16x32_bf16 v[28:31], v[140:143], v[44:47], v[28:31]\n"
    "ds_read_b64_tr_b16 v[40:41], v13 offset:16384\n"
    "v_mfma_f32_16x16x32_bf16 v[240:243], v[128:131], v[48:51], v[240:243]\n"
    "ds_read_b64_tr_b16 v[42:43], v13 offset:16896\n"
    "v_mfma_f32_16x16x32_bf16 v[240:243], v[136:139], v[52:55], v[240:243]\n"
    "ds_read_b64_tr_b16 v[44:45], v13 offset:20480\n"
    "v_mfma_f32_16x16x32_bf16 v[244:247], v[132:135], v[48:51], v[244:247]\n"
    "ds_read_b64_tr_b16 v[46:47], v13 offset:20992\n"
    "v_mfma_f32_16x16x32_bf16 v[244:247], v[140:143], v[52:55], v[244:247]\n"
    "ds_read_b64_tr_b16 v[48:49], v14 offset:16384\n"
    "v_mfma_f32_16x16x32_bf16 v[248:251], v[128:131], v[56:59], v[248:251]\n"
    "ds_read_b64_tr_b16 v[50:51], v14 offset:16896\n"
    "v_mfma_f32_16x16x32_bf16 v[248:251], v[136:139], v[60:63], v[248:251]\n"
    "ds_read_b64_tr_b16 v[52:53], v14 offset:20480\n"
    "v_mfma_f32_16x16x32_bf16 v[252:255], v[132:135], v[56:59], v[252:255]\n"
    "ds_read_b64_tr_b16 v[54:55], v14 offset:20992\n"
    "v_mfma_f32_16x16x32_bf16 v[252:255], v[140:143], v[60:63], v[252:255]\n"
    "ds_read_b64_tr_b16 v[56:57], v15 offset:16384\n"
    "ds_read_b64_tr_b16 v[58:59], v15 offset:16896\n"
    "ds_read_b64_tr_b16 v[60:61], v15 offset:20480\n"
    "s_waitcnt lgkmcnt(11)\n"
    "v_mfma_f32_16x16x32_bf16 v[16:19], v[144:147], v[32:35], v[16:19]\n"
    "ds_read_b64_tr_b16 v[62:63], v15 offset:20992\n"
    "v_mfma_f32_16x16x32_bf16 v[16:19], v[152:155], v[36:39], v[16:19]\n"
    "v_mfma_f32_16x16x32_bf16 v[20:23], v[148:151], v[32:35], v[20:23]\n"
    "v_mfma_f32_16x16x32_bf16 v[20:23], v[156:159], v[36:39], v[20:23]\n"
    "s_waitcnt lgkmcnt(0)\n"
    "s_barrier\n"
    "ds_read_b128 v[160:163], v8 offset:0\n"
    "ds_read_b128 v[164:167], v8 offset:2048\n"
    "ds_read_b128 v[168:171], v9 offset:0\n"
    "ds_read_b128 v[172:175], v9 offset:2048\n"
    "ds_read_b64_tr_b16 v[32:33], v12 offset:0\n"
    "v_mfma_f32_16x16x32_bf16 v[24:27], v[144:147], v[40:43], v[24:27]\n"
    "v_mfma_f32_16x16x32_bf16 v[24:27], v[152:155], v[44:47], v[24:27]\n"
    "ds_read_b64_tr_b16 v[34:35], v12 offset:512\n"
    "ds_read_b64_tr_b16 v[36:37], v12 offset:4096\n"
    "v_mfma_f32_16x16x32_bf16 v[28:31], v[148:151], v[40:43], v[28:31]\n"
    "ds_read_b64_tr_b16 v[38:39], v12 offset:4608\n"
    "v_mfma_f32_16x16x32_bf16 v[28:31], v[156:159], v[44:47], v[28:31]\n"
    "ds_read_b64_tr_b16 v[40:41], v13 offset:0\n"
    "v_mfma_f32_16x16x32_bf16 v[240:243], v[144:147], v[48:51], v[240:243]\n"
    "ds_read_b64_tr_b16 v[42:43], v13 offset:512\n"
    "v_mfma_f32_16x16x32_bf16 v[240:243], v[152:155], v[52:55], v[240:243]\n"
    "ds_read_b64_tr_b16 v[44:45], v13 offset:4096\n"
    "v_mfma_f32_16x16x32_bf16 v[244:247], v[148:151], v[48:51], v[244:247]\n"
    "ds_read_b64_tr_b16 v[46:47], v13 offset:4608\n"
    "v_mfma_f32_16x16x32_bf16 v[244:247], v[156:159], v[52:55], v[244:247]\n"
    "ds_read_b64_tr_b16 v[48:49], v14 offset:0\n"
    "v_mfma_f32_16x16x32_bf16 v[248:251], v[144:147], v[56:59], v[248:251]\n"
    "ds_read_b64_tr_b16 v[50:51], v14 offset:512\n"
    "v_mfma_f32_16x16x32_bf16 v[248:251], v[152:155], v[60:63], v[248:251]\n"
    "ds_read_b64_tr_b16 v[52:53], v14 offset:4096\n"
    "v_mfma_f32_16x16x32_bf16 v[252:255], v[148:151], v[56:59], v[252:255]\n"
    "ds_read_b64_tr_b16 v[54:55], v14 offset:4608\n"
    "v_mfma_f32_16x16x32_bf16 v[252:255], v[156:159], v[60:63], v[252:255]\n"
    "ds_read_b64_tr_b16 v[56:57], v15 offset:0\n"
    "ds_read_b64_tr_b16 v[58:59], v15 offset:512\n"
    "ds_read_b64_tr_b16 v[60:61], v15 offset:4096\n"
    "s_waitcnt lgkmcnt(11)\n"
    "v_mfma_f32_16x16x32_bf16 v[16:19], v[160:163], v[32:35], v[16:19]\n"
    "ds_read_b64_tr_b16 v[62:63], v15 offset:4608\n"
    "v_mfma_f32_16x16x32_bf16 v[16:19], v[168:171], v[36:39], v[16:19]\n"
    "v_mfma_f32_16x16x32_bf16 v[20:23], v[164:167], v[32:35], v[20:23]\n"
    "v_mfma_f32_16x16x32_bf16 v[20:23], v[172:175], v[36:39], v[20:23]\n"
    "s_setprio 3\n"
    "s_waitcnt vmcnt(0)\n"
    "s_waitcnt lgkmcnt(0)\n"
    "s_barrier\n"
    "v_mfma_f32_16x16x32_bf16 v[24:27], v[160:163], v[40:43], v[24:27]\n"
    "v_mfma_f32_16x16x32_bf16 v[24:27], v[168:171], v[44:47], v[24:27]\n"
    "v_mfma_f32_16x16x32_bf16 v[28:31], v[164:167], v[40:43], v[28:31]\n"
    "v_mfma_f32_16x16x32_bf16 v[28:31], v[172:175], v[44:47], v[28:31]\n"
    "v_mfma_f32_16x16x32_bf16 v[240:243], v[160:163], v[48:51], v[240:243]\n"
    "v_mfma_f32_16x16x32_bf16 v[240:243], v[168:171], v[52:55], v[240:243]\n"
    "v_mfma_f32_16x16x32_bf16 v[244:247], v[164:167], v[48:51], v[244:247]\n"
    "v_mfma_f32_16x16x32_bf16 v[244:247], v[172:175], v[52:55], v[244:247]\n"
    "v_mfma_f32_16x16x32_bf16 v[248:251], v[160:163], v[56:59], v[248:251]\n"
    "v_mfma_f32_16x16x32_bf16 v[248:251], v[168:171], v[60:63], v[248:251]\n"
    "v_mfma_f32_16x16x32_bf16 v[252:255], v[164:167], v[56:59], v[252:255]\n"
    "v_mfma_f32_16x16x32_bf16 v[252:255], v[172:175], v[60:63], v[252:255]\n"
    ".Lkv_done:\n"
    "v_mov_b32 v212, v208\n"
    "v_xor_b32 v12, 98304, v6\n"
    "v_xor_b32 v13, 32, v12\n"
    "s_setprio 3\n"
    "ds_read_b64_tr_b16 v[32:33], v12 offset:0\n"
    "ds_read_b64_tr_b16 v[34:35], v12 offset:512\n"
    "ds_read_b64_tr_b16 v[36:37], v12 offset:4096\n"
    "ds_read_b64_tr_b16 v[38:39], v12 offset:4608\n"
    "ds_read_b64_tr_b16 v[40:41], v12 offset:8192\n"
    "ds_read_b64_tr_b16 v[42:43], v12 offset:8704\n"
    "ds_read_b64_tr_b16 v[44:45], v12 offset:12288\n"
    "ds_read_b64_tr_b16 v[46:47], v12 offset:12800\n"
    "ds_read_b64_tr_b16 v[48:49], v12 offset:16384\n"
    "ds_read_b64_tr_b16 v[50:51], v12 offset:16896\n"
    "ds_read_b64_tr_b16 v[52:53], v12 offset:20480\n"
    "ds_read_b64_tr_b16 v[54:55], v12 offset:20992\n"
    "v_cvt_pk_bf16_f32 v176, v16, v17\n"
    "v_cvt_pk_bf16_f32 v177, v18, v19\n"
    "v_cvt_pk_bf16_f32 v178, v20, v21\n"
    "v_cvt_pk_bf16_f32 v179, v22, v23\n"
    "s_nop 0\n"
    "v_permlane16_swap_b32 v176, v178\n"
    "v_permlane16_swap_b32 v177, v179\n"
    "buffer_store_dwordx4 v[176:179], v230, s[16:19], s52 offen sc1\n"
    "v_cvt_pk_bf16_f32 v180, v24, v25\n"
    "v_cvt_pk_bf16_f32 v181, v26, v27\n"
    "v_cvt_pk_bf16_f32 v182, v28, v29\n"
    "v_cvt_pk_bf16_f32 v183, v30, v31\n"
    "s_nop 0\n"
    "v_permlane16_swap_b32 v180, v182\n"
    "v_permlane16_swap_b32 v181, v183\n"
    "buffer_store_dwordx4 v[180:183], v231, s[16:19], s52 offen sc1\n"
    "v_mfma_f32_16x16x32_bf16 v[16:19], v[212:215], v[216:219], 0\n"
    "v_mfma_f32_16x16x32_bf16 v[20:23], v[212:215], v[220:223], 0\n"
    "v_mfma_f32_16x16x32_bf16 v[24:27], v[212:215], v[216:219], 0\n"
    "v_mfma_f32_16x16x32_bf16 v[28:31], v[212:215], v[220:223], 0\n"
    "s_waitcnt lgkmcnt(8)\n"
    "v_mfma_f32_16x16x32_bf16 v[16:19], v[32:35], v[64:67], v[16:19]\n"
    "ds_read_b64_tr_b16 v[56:57], v12 offset:24576\n"
    "v_mfma_f32_16x16x32_bf16 v[16:19], v[36:39], v[72:75], v[16:19]\n"
    "ds_read_b64_tr_b16 v[58:59], v12 offset:25088\n"
    "v_mfma_f32_16x16x32_bf16 v[24:27], v[32:35], v[68:71], v[24:27]\n"
    "ds_read_b64_tr_b16 v[60:61], v12 offset:28672\n"
    "v_mfma_f32_16x16x32_bf16 v[24:27], v[36:39], v[76:79], v[24:27]\n"
    "ds_read_b64_tr_b16 v[62:63], v12 offset:29184\n"
    "v_cvt_pk_bf16_f32 v184, v240, v241\n"
    "v_cvt_pk_bf16_f32 v185, v242, v243\n"
    "v_cvt_pk_bf16_f32 v186, v244, v245\n"
    "v_cvt_pk_bf16_f32 v187, v246, v247\n"
    "s_nop 0\n"
    "v_permlane16_swap_b32 v184, v186\n"
    "v_permlane16_swap_b32 v185, v187\n"
    "buffer_store_dwordx4 v[184:187], v232, s[16:19], s52 offen sc1\n"
    "s_waitcnt lgkmcnt(8)\n"
    "v_mfma_f32_16x16x32_bf16 v[24:27], v[40:43], v[84:87], v[24:27]\n"
    "ds_read_b64_tr_b16 v[32:33], v12 offset:32768\n"
    "v_mfma_f32_16x16x32_bf16 v[24:27], v[44:47], v[92:95], v[24:27]\n"
    "ds_read_b64_tr_b16 v[34:35], v12 offset:33280\n"
    "s_setprio 3\n"
    "v_mfma_f32_16x16x32_bf16 v[16:19], v[40:43], v[80:83], v[16:19]\n"
    "ds_read_b64_tr_b16 v[36:37], v12 offset:36864\n"
    "v_mfma_f32_16x16x32_bf16 v[16:19], v[44:47], v[88:91], v[16:19]\n"
    "ds_read_b64_tr_b16 v[38:39], v12 offset:37376\n"
    "v_cvt_pk_bf16_f32 v188, v248, v249\n"
    "v_cvt_pk_bf16_f32 v189, v250, v251\n"
    "v_cvt_pk_bf16_f32 v190, v252, v253\n"
    "v_cvt_pk_bf16_f32 v191, v254, v255\n"
    "s_nop 0\n"
    "v_permlane16_swap_b32 v188, v190\n"
    "v_permlane16_swap_b32 v189, v191\n"
    "buffer_store_dwordx4 v[188:191], v233, s[16:19], s52 offen sc1\n"
    "s_waitcnt lgkmcnt(8)\n"
    "v_mfma_f32_16x16x32_bf16 v[16:19], v[48:51], v[96:99], v[16:19]\n"
    "ds_read_b64_tr_b16 v[40:41], v12 offset:40960\n"
    "v_mfma_f32_16x16x32_bf16 v[16:19], v[52:55], v[104:107], v[16:19]\n"
    "ds_read_b64_tr_b16 v[42:43], v12 offset:41472\n"
    "v_mfma_f32_16x16x32_bf16 v[24:27], v[48:51], v[100:103], v[24:27]\n"
    "ds_read_b64_tr_b16 v[44:45], v12 offset:45056\n"
    "v_mfma_f32_16x16x32_bf16 v[24:27], v[52:55], v[108:111], v[24:27]\n"
    "ds_read_b64_tr_b16 v[46:47], v12 offset:45568\n"
    "s_cmp_lt_u32 s69, 9\n"
    "s_cbranch_scc1 .La_nopad\n"
    "s_mov_b64 exec, s[50:51]\n"
    "buffer_store_dwordx4 v[192:195], v234, s[16:19], s52 offen offset:448 sc1\n"
    "s_mov_b64 exec, -1\n"
    ".La_nopad:\n"
    "v_mov_b32 v224, 2\n"
    "ds_write_b32 v0, v224\n"
    "s_waitcnt lgkmcnt(9)\n"
    "v_mfma_f32_16x16x32_bf16 v[24:27], v[56:59], v[116:119], v[24:27]\n"
    "ds_read_b64_tr_b16 v[48:49], v12 offset:49152\n"
    "v_mfma_f32_16x16x32_bf16 v[24:27], v[60:63], v[124:127], v[24:27]\n"
    "ds_read_b64_tr_b16 v[50:51], v12 offset:49664\n"
    "v_mfma_f32_16x16x32_bf16 v[16:19], v[56:59], v[112:115], v[16:19]\n"
    "ds_read_b64_tr_b16 v[52:53], v12 offset:53248\n"
    "v_mfma_f32_16x16x32_bf16 v[16:19], v[60:63], v[120:123], v[16:19]\n"
    "ds_read_b64_tr_b16 v[54:55], v12 offset:53760\n"
    "s_waitcnt lgkmcnt(9)\n"
    "v_mfma_f32_16x16x32_bf16 v[16:19], v[32:35], v[128:131], v[16:19]\n"
    "ds_read_b64_tr_b16 v[56:57], v13 offset:0\n"
    "v_mfma_f32_16x16x32_bf16 v[16:19], v[36:39], v[136:139], v[16:19]\n"
    "ds_read_b64_tr_b16 v[58:59], v13 offset:512\n"
    "v_mfma_f32_16x16x32_bf16 v[24:27], v[32:35], v[132:135], v[24:27]\n"
    "ds_read_b64_tr_b16 v[60:61], v13 offset:4096\n"
    "v_mfma_f32_16x16x32_bf16 v[24:27], v[36:39], v[140:143], v[24:27]\n"
    "ds_read_b64_tr_b16 v[62:63], v13 offset:4608\n"
    "s_waitcnt lgkmcnt(9)\n"
    "v_mfma_f32_16x16x32_bf16 v[24:27], v[40:43], v[148:151], v[24:27]\n"
    "ds_read_b64_tr_b16 v[32:33], v13 offset:8192\n"
    "v_mfma_f32_16x16x32_bf16 v[24:27], v[44:47], v[156:159], v[24:27]\n"
    "ds_read_b64_tr_b16 v[34:35], v13 offset:8704\n"
    "v_mfma_f32_16x16x32_bf16 v[16:19], v[40:43], v[144:147], v[16:19]\n"
    "ds_read_b64_tr_b16 v[36:37], v13 offset:12288\n"
    "v_mfma_f32_16x16x32_bf16 v[16:19], v[44:47], v[152:155], v[16:19]\n"
    "ds_read_b64_tr_b16 v[38:39], v13 offset:12800\n"
    "s_waitcnt lgkmcnt(8)\n"
    "v_mfma_f32_16x16x32_bf16 v[16:19], v[48:51], v[160:163], v[16:19]\n"
    "ds_read_b64_tr_b16 v[40:41], v13 offset:16384\n"
    "v_mfma_f32_16x16x32_bf16 v[16:19], v[52:55], v[168:171], v[16:19]\n"
    "ds_read_b64_tr_b16 v[42:43], v13 offset:16896\n"
    "v_mfma_f32_16x16x32_bf16 v[24:27], v[48:51], v[164:167], v[24:27]\n"
    "ds_read_b64_tr_b16 v[44:45], v13 offset:20480\n"
    "v_mfma_f32_16x16x32_bf16 v[24:27], v[52:55], v[172:175], v[24:27]\n"
    "ds_read_b64_tr_b16 v[46:47], v13 offset:20992\n"
    "s_waitcnt lgkmcnt(8)\n"
    "v_mfma_f32_16x16x32_bf16 v[28:31], v[56:59], v[68:71], v[28:31]\n"
    "ds_read_b64_tr_b16 v[48:49], v13 offset:24576\n"
    "v_mfma_f32_16x16x32_bf16 v[28:31], v[60:63], v[76:79], v[28:31]\n"
    "ds_read_b64_tr_b16 v[50:51], v13 offset:25088\n"
    "v_mfma_f32_16x16x32_bf16 v[20:23], v[56:59], v[64:67], v[20:23]\n"
    "ds_read_b64_tr_b16 v[52:53], v13 offset:28672\n"
    "v_mfma_f32_16x16x32_bf16 v[20:23], v[60:63], v[72:75], v[20:23]\n"
    "ds_read_b64_tr_b16 v[54:55], v13 offset:29184\n"
    "s_waitcnt lgkmcnt(8)\n"
    "v_mfma_f32_16x16x32_bf16 v[20:23], v[32:35], v[80:83], v[20:23]\n"
    "ds_read_b64_tr_b16 v[56:57], v13 offset:32768\n"
    "v_mfma_f32_16x16x32_bf16 v[20:23], v[36:39], v[88:91], v[20:23]\n"
    "ds_read_b64_tr_b16 v[58:59], v13 offset:33280\n"
    "v_mfma_f32_16x16x32_bf16 v[28:31], v[32:35], v[84:87], v[28:31]\n"
    "ds_read_b64_tr_b16 v[60:61], v13 offset:36864\n"
    "v_mfma_f32_16x16x32_bf16 v[28:31], v[36:39], v[92:95], v[28:31]\n"
    "ds_read_b64_tr_b16 v[62:63], v13 offset:37376\n"
    "s_waitcnt lgkmcnt(8)\n"
    "v_mfma_f32_16x16x32_bf16 v[28:31], v[40:43], v[100:103], v[28:31]\n"
    "ds_read_b64_tr_b16 v[32:33], v13 offset:40960\n"
    "v_mfma_f32_16x16x32_bf16 v[28:31], v[44:47], v[108:111], v[28:31]\n"
    "ds_read_b64_tr_b16 v[34:35], v13 offset:41472\n"
    "v_mfma_f32_16x16x32_bf16 v[20:23], v[40:43], v[96:99], v[20:23]\n"
    "ds_read_b64_tr_b16 v[36:37], v13 offset:45056\n"
    "v_mfma_f32_16x16x32_bf16 v[20:23], v[44:47], v[104:107], v[20:23]\n"
    "ds_read_b64_tr_b16 v[38:39], v13 offset:45568\n"
    "s_waitcnt lgkmcnt(8)\n"
    "v_mfma_f32_16x16x32_bf16 v[20:23], v[48:51], v[112:115], v[20:23]\n"
    "ds_read_b64_tr_b16 v[40:41], v13 offset:49152\n"
    "v_mfma_f32_16x16x32_bf16 v[20:23], v[52:55], v[120:123], v[20:23]\n"
    "ds_read_b64_tr_b16 v[42:43], v13 offset:49664\n"
    "v_mfma_f32_16x16x32_bf16 v[28:31], v[48:51], v[116:119], v[28:31]\n"
    "ds_read_b64_tr_b16 v[44:45], v13 offset:53248\n"
    "v_mfma_f32_16x16x32_bf16 v[28:31], v[52:55], v[124:127], v[28:31]\n"
    "ds_read_b64_tr_b16 v[46:47], v13 offset:53760\n"
    "s_waitcnt lgkmcnt(8)\n"
    "v_mfma_f32_16x16x32_bf16 v[28:31], v[56:59], v[132:135], v[28:31]\n"
    "v_mfma_f32_16x16x32_bf16 v[28:31], v[60:63], v[140:143], v[28:31]\n"
    "v_mfma_f32_16x16x32_bf16 v[20:23], v[56:59], v[128:131], v[20:23]\n"
    "v_mfma_f32_16x16x32_bf16 v[20:23], v[60:63], v[136:139], v[20:23]\n"
    "s_waitcnt lgkmcnt(4)\n"
    "v_mfma_f32_16x16x32_bf16 v[20:23], v[32:35], v[144:147], v[20:23]\n"
    "v_mfma_f32_16x16x32_bf16 v[20:23], v[36:39], v[152:155], v[20:23]\n"
    "v_mfma_f32_16x16x32_bf16 v[28:31], v[32:35], v[148:151], v[28:31]\n"
    "v_mfma_f32_16x16x32_bf16 v[28:31], v[36:39], v[156:159], v[28:31]\n"
    "s_waitcnt lgkmcnt(0)\n"
    "v_mfma_f32_16x16x32_bf16 v[28:31], v[40:43], v[164:167], v[28:31]\n"
    "v_mfma_f32_16x16x32_bf16 v[28:31], v[44:47], v[172:175], v[28:31]\n"
    "v_mfma_f32_16x16x32_bf16 v[20:23], v[40:43], v[160:163], v[20:23]\n"
    "v_mfma_f32_16x16x32_bf16 v[20:23], v[44:47], v[168:171], v[20:23]\n"
    "s_setprio 1\n"
    "s_lshl_b32 s53, s47, 11\n"
    "s_add_u32 s53, s53, 90112\n"
    "v_add_u32 v230, s53, v2\n"
    "v_mov_b32 v12, v236\n"
    "v_mov_b32 v13, v237\n"
    "s_setprio 2\n"
    "v_mul_f32 v192, s0, v16\n"
    "v_mul_f32 v193, s0, v17\n"
    "v_mul_f32 v194, s0, v18\n"
    "v_mul_f32 v195, s0, v19\n"
    "v_mul_f32 v196, s0, v20\n"
    "v_mul_f32 v197, s0, v21\n"
    "v_mul_f32 v198, s0, v22\n"
    "v_mul_f32 v199, s0, v23\n"
    "v_exp_f32 v192, v192\n"
    "v_exp_f32 v193, v193\n"
    "v_exp_f32 v194, v194\n"
    "v_exp_f32 v195, v195\n"
    "v_exp_f32 v196, v196\n"
    "v_exp_f32 v197, v197\n"
    "v_exp_f32 v198, v198\n"
    "v_exp_f32 v199, v199\n"
    "v_add_f32 v192, 1.0, v192\n"
    "v_add_f32 v193, 1.0, v193\n"
    "v_add_f32 v194, 1.0, v194\n"
    "v_add_f32 v195, 1.0, v195\n"
    "v_add_f32 v196, 1.0, v196\n"
    "v_add_f32 v197, 1.0, v197\n"
    "v_add_f32 v198, 1.0, v198\n"
    "v_add_f32 v199, 1.0, v199\n"
    "s_waitcnt vmcnt(0)\n"
    "v_mov_b64 v[224:225], s[48:49]\n"
    "s_mov_b64 exec, 1\n"
    "buffer_store_dwordx2 v[224:225], off, s[16:19], s1 sc1\n"
    "s_mov_b64 exec, -1\n"
    "v_mov_b32 v226, 3\n"
    "ds_write_b32 v0, v226\n"
    "v_fma_f32 v192, v192, v192, 1.0\n"
    "v_fma_f32 v193, v193, v193, 1.0\n"
    "v_fma_f32 v194, v194, v194, 1.0\n"
    "v_fma_f32 v195, v195, v195, 1.0\n"
    "v_fma_f32 v196, v196, v196, 1.0\n"
    "v_fma_f32 v197, v197, v197, 1.0\n"
    "v_fma_f32 v198, v198, v198, 1.0\n"
    "v_fma_f32 v199, v199, v199, 1.0\n"
    "v_rcp_f32 v192, v192\n"
    "v_rcp_f32 v193, v193\n"
    "v_rcp_f32 v194, v194\n"
    "v_rcp_f32 v195, v195\n"
    "v_rcp_f32 v196, v196\n"
    "v_rcp_f32 v197, v197\n"
    "v_rcp_f32 v198, v198\n"
    "v_rcp_f32 v199, v199\n"
    "v_fma_f32 v192, v192, -2.0, 1.0\n"
    "v_fma_f32 v193, v193, -2.0, 1.0\n"
    "v_fma_f32 v194, v194, -2.0, 1.0\n"
    "v_fma_f32 v195, v195, -2.0, 1.0\n"
    "v_fma_f32 v196, v196, -2.0, 1.0\n"
    "v_fma_f32 v197, v197, -2.0, 1.0\n"
    "v_fma_f32 v198, v198, -2.0, 1.0\n"
    "v_fma_f32 v199, v199, -2.0, 1.0\n"
    "v_mul_f32 v192, v192, v16\n"
    "v_mul_f32 v193, v193, v17\n"
    "v_mul_f32 v194, v194, v18\n"
    "v_mul_f32 v195, v195, v19\n"
    "v_mul_f32 v196, v196, v20\n"
    "v_mul_f32 v197, v197, v21\n"
    "v_mul_f32 v198, v198, v22\n"
    "v_mul_f32 v199, v199, v23\n"
    "v_cvt_pk_bf16_f32 v176, v192, v193\n"
    "v_cvt_pk_bf16_f32 v177, v194, v195\n"
    "v_cvt_pk_bf16_f32 v178, v196, v197\n"
    "v_cvt_pk_bf16_f32 v179, v198, v199\n"
    "v_mul_f32 v200, s0, v24\n"
    "v_mul_f32 v201, s0, v25\n"
    "v_mul_f32 v202, s0, v26\n"
    "v_mul_f32 v203, s0, v27\n"
    "v_mul_f32 v204, s0, v28\n"
    "v_mul_f32 v205, s0, v29\n"
    "v_mul_f32 v206, s0, v30\n"
    "v_mul_f32 v207, s0, v31\n"
    "v_exp_f32 v200, v200\n"
    "v_exp_f32 v201, v201\n"
    "v_exp_f32 v202, v202\n"
    "v_exp_f32 v203, v203\n"
    "v_exp_f32 v204, v204\n"
    "v_exp_f32 v205, v205\n"
    "v_exp_f32 v206, v206\n"
    "v_exp_f32 v207, v207\n"
    "v_add_f32 v200, 1.0, v200\n"
    "v_add_f32 v201, 1.0, v201\n"
    "v_add_f32 v202, 1.0, v202\n"
    "v_add_f32 v203, 1.0, v203\n"
    "v_add_f32 v204, 1.0, v204\n"
    "v_add_f32 v205, 1.0, v205\n"
    "v_add_f32 v206, 1.0, v206\n"
    "v_add_f32 v207, 1.0, v207\n"
    "v_fma_f32 v200, v200, v200, 1.0\n"
    "v_fma_f32 v201, v201, v201, 1.0\n"
    "v_fma_f32 v202, v202, v202, 1.0\n"
    "v_fma_f32 v203, v203, v203, 1.0\n"
    "v_fma_f32 v204, v204, v204, 1.0\n"
    "v_fma_f32 v205, v205, v205, 1.0\n"
    "v_fma_f32 v206, v206, v206, 1.0\n"
    "v_fma_f32 v207, v207, v207, 1.0\n"
    "v_rcp_f32 v200, v200\n"
    "v_rcp_f32 v201, v201\n"
    "v_rcp_f32 v202, v202\n"
    "v_rcp_f32 v203, v203\n"
    "v_rcp_f32 v204, v204\n"
    "v_rcp_f32 v205, v205\n"
    "v_rcp_f32 v206, v206\n"
    "v_rcp_f32 v207, v207\n"
    "v_fma_f32 v200, v200, -2.0, 1.0\n"
    "v_fma_f32 v201, v201, -2.0, 1.0\n"
    "v_fma_f32 v202, v202, -2.0, 1.0\n"
    "v_fma_f32 v203, v203, -2.0, 1.0\n"
    "v_fma_f32 v204, v204, -2.0, 1.0\n"
    "v_fma_f32 v205, v205, -2.0, 1.0\n"
    "v_fma_f32 v206, v206, -2.0, 1.0\n"
    "v_fma_f32 v207, v207, -2.0, 1.0\n"
    "v_mul_f32 v200, v200, v24\n"
    "v_mul_f32 v201, v201, v25\n"
    "v_mul_f32 v202, v202, v26\n"
    "v_mul_f32 v203, v203, v27\n"
    "v_mul_f32 v204, v204, v28\n"
    "v_mul_f32 v205, v205, v29\n"
    "v_mul_f32 v206, v206, v30\n"
    "v_mul_f32 v207, v207, v31\n"
    "v_cvt_pk_bf16_f32 v180, v200, v201\n"
    "v_cvt_pk_bf16_f32 v181, v202, v203\n"
    "v_cvt_pk_bf16_f32 v182, v204, v205\n"
    "v_cvt_pk_bf16_f32 v183, v206, v207\n"
    "ds_write_b128 v230, v[176:179] offset:0\n"
    "ds_write_b128 v230, v[180:183] offset:1024\n"
    "v_mov_b32 v225, 4\n"
    "ds_write_b32 v0, v225\n"
    "buffer_load_dwordx2 v[22:23], v13, s[16:19], s73 offen sc1\n"
    "buffer_load_dwordx2 v[20:21], v12, s[16:19], s72 offen sc1\n"
    "v_mov_b32 v28, 0\n"
    "v_mov_b32 v29, 0\n"
    "v_mov_b32 v30, 0\n"
    "v_mov_b32 v31, 0\n"
    "v_mov_b32 v16, 0xdddd\n"
    "v_mov_b32 v17, 6\n"
    "v_mov_b32 v26, 163328\n"
    "s_waitcnt lgkmcnt(0)\n"
    "v_mfma_f32_32x32x16_bf16 v[192:207], v[28:31], v[28:31], 0\n"
    "s_mul_i32 s55, s47, 12288\n"
    "s_add_u32 s55, s55, 57344\n"
    "v_add_u32 v8, s55, v2\n"
    "s_xor_b32 s55, s47, 3\n"
    "s_mul_i32 s55, s55, 12288\n"
    "s_add_u32 s55, s55, 57344\n"
    "v_mfma_f32_32x32x16_bf16 v[208:223], v[28:31], v[28:31], 0\n"
    "v_add_u32 v9, s55, v2\n"
    "s_xor_b32 s55, s47, 2\n"
    "s_mul_i32 s55, s55, 12288\n"
    "s_add_u32 s55, s55, 61440\n"
    "v_add_u32 v10, s55, v2\n"
    "s_xor_b32 s55, s47, 1\n"
    "v_mfma_f32_32x32x16_bf16 v[224:239], v[28:31], v[28:31], 0\n"
    "s_mul_i32 s55, s55, 12288\n"
    "s_add_u32 s55, s55, 65536\n"
    "v_add_u32 v11, s55, v2\n"
    "s_lshl_b32 s55, s62, 4\n"
    "s_add_u32 s55, s55, s45\n"
    "s_lshl_b32 s71, s55, 5\n"
    "v_mfma_f32_32x32x16_bf16 v[240:255], v[28:31], v[28:31], 0\n"
    "s_add_u32 s71, s71, 442368\n"
    "s_lshl_b32 s70, s55, 11\n"
    "s_add_u32 s70, s70, 5242880\n"
    "s_and_b32 s55, s45, 1\n"
    "v_cmp_le_u32 vcc, s55, v3\n"
    "v_cmp_ge_u32 s[50:51], s55, v3\n"
    "s_nop 0\n"
    "v_cndmask_b32 v14, 0, -1, vcc\n"
    "v_cndmask_b32 v15, 0, -1, s[50:51]\n"
    "s_mov_b32 s63, 0x20000\n"
    ".La_pq:\n"
    "s_waitcnt vmcnt(0)\n"
    "v_cmp_ne_u64 vcc, s[48:49], v[22:23]\n"
    "v_cmp_ne_u64 s[50:51], s[48:49], v[20:21]\n"
    "s_or_b64 vcc, vcc, s[50:51]\n"
    "s_cbranch_scc0 .La_ready\n"
    "s_sleep 2\n"
    "buffer_load_dwordx2 v[22:23], v13, s[16:19], s73 offen sc1\n"
    "buffer_load_dwordx2 v[20:21], v12, s[16:19], s72 offen sc1\n"
    "s_sub_u32 s63, s63, 1\n"
    "s_cbranch_scc0 .La_pq\n"
    "s_waitcnt vmcnt(0)\n"
    ".La_ready:\n"
    "s_mov_b32 s54, 0\n"
    "s_mul_i32 s52, s54, 61440\n"
    "buffer_load_dwordx4 v[64:67], v4, s[40:43], s52 offen offset:0 sc1\n"
    "buffer_load_dwordx4 v[68:71], v4, s[40:43], s52 offen offset:1024 sc1\n"
    "s_xor_b32 s53, s62, 3\n"
    "s_mul_i32 s53, s53, 28672\n"
    "s_add_u32 s53, s53, 14680064\n"
    "buffer_load_dwordx4 v[32:35], v4, s[16:19], s53 offen offset:0 sc1\n"
    "buffer_load_dwordx4 v[36:39], v4, s[16:19], s53 offen offset:1024 sc1\n"
    "s_xor_b32 s53, s62, 2\n"
    "s_mul_i32 s53, s53, 28672\n"
    "s_add_u32 s53, s53, 14680064\n"
    "buffer_load_dwordx4 v[40:43], v4, s[16:19], s53 offen offset:0 sc1\n"
    "buffer_load_dwordx4 v[44:47], v4, s[16:19], s53 offen offset:1024 sc1\n"
    "s_xor_b32 s53, s62, 1\n"
    "s_mul_i32 s53, s53, 28672\n"
    "s_add_u32 s53, s53, 14680064\n"
    "buffer_load_dwordx4 v[48:51], v4, s[16:19], s53 offen offset:0 sc1\n"
    "buffer_load_dwordx4 v[52:55], v4, s[16:19], s53 offen offset:1024 sc1\n"
    "buffer_load_dwordx4 v[72:75], v5, s[40:43], s52 offen offset:2048 sc1\n"
    "buffer_load_dwordx4 v[76:79], v5, s[40:43], s52 offen offset:3072 sc1\n"
    "s_xor_b32 s53, s62, 0\n"
    "s_mul_i32 s53, s53, 28672\n"
    "s_add_u32 s53, s53, 14680064\n"
    "buffer_load_dwordx4 v[56:59], v4, s[16:19], s53 offen offset:0 sc1\n"
    "buffer_load_dwordx4 v[60:63], v4, s[16:19], s53 offen offset:1024 sc1\n"
    "s_add_u32 s52, s52, 61440\n"
    "buffer_load_dwordx4 v[80:83], v4, s[40:43], s52 offen offset:0 sc1\n"
    "buffer_load_dwordx4 v[84:87], v4, s[40:43], s52 offen offset:1024 sc1\n"
    "v_mov_b32 v18, 5\n"
    "ds_write_b32 v0, v18\n"
    "s_setprio 1\n"
    "s_waitcnt vmcnt(10)\n"
    "v_and_b32 v32, v14, v32\n"
    "v_and_b32 v33, v14, v33\n"
    "v_and_b32 v38, v15, v38\n"
    "v_and_b32 v39, v15, v39\n"
    "v_mfma_f32_32x32x16_bf16 v[128:143], v[64:67], v[32:35], 0\n"
    "s_nop 0\n"
    "v_mfma_f32_32x32x16_bf16 v[128:143], v[68:71], v[36:39], v[128:143]\n"
    "s_waitcnt vmcnt(8)\n"
    "v_and_b32 v40, v14, v40\n"
    "v_and_b32 v41, v14, v41\n"
    "v_and_b32 v46, v15, v46\n"
    "v_and_b32 v47, v15, v47\n"
    "v_mfma_f32_32x32x16_bf16 v[144:159], v[64:67], v[40:43], 0\n"
    "s_nop 5\n"
    "v_exp_f32 v128, v128\n"
    "v_exp_f32 v129, v129\n"
    "v_exp_f32 v130, v130\n"
    "v_exp_f32 v131, v131\n"
    "v_mfma_f32_32x32x16_bf16 v[144:159], v[68:71], v[44:47], v[144:159]\n"
    "buffer_load_dwordx4 v[88:91], v5, s[40:43], s52 offen offset:2048 sc1\n"
    "v_exp_f32 v132, v132\n"
    "v_exp_f32 v133, v133\n"
    "v_exp_f32 v134, v134\n"
    "v_exp_f32 v135, v135\n"
    "buffer_load_dwordx4 v[92:95], v5, s[40:43], s52 offen offset:3072 sc1\n"
    "v_exp_f32 v136, v136\n"
    "v_exp_f32 v137, v137\n"
    "v_exp_f32 v138, v138\n"
    "v_exp_f32 v139, v139\n"
    "s_add_u32 s52, s52, 61440\n"
    "s_waitcnt vmcnt(8)\n"
    "v_and_b32 v48, v14, v48\n"
    "v_and_b32 v49, v14, v49\n"
    "v_and_b32 v54, v15, v54\n"
    "v_and_b32 v55, v15, v55\n"
    "v_exp_f32 v140, v140\n"
    "v_exp_f32 v141, v141\n"
    "v_exp_f32 v142, v142\n"
    "v_exp_f32 v143, v143\n"
    "v_mfma_f32_32x32x16_bf16 v[160:175], v[64:67], v[48:51], 0\n"
    "v_exp_f32 v144, v144\n"
    "v_exp_f32 v145, v145\n"
    "v_exp_f32 v146, v146\n"
    "v_exp_f32 v147, v147\n"
    "v_mfma_f32_32x32x16_bf16 v[160:175], v[68:71], v[52:55], v[160:175]\n"
    "buffer_load_dwordx4 v[96:99], v4, s[40:43], s52 offen offset:0 sc1\n"
    "v_exp_f32 v148, v148\n"
    "v_exp_f32 v149, v149\n"
    "v_exp_f32 v150, v150\n"
    "v_exp_f32 v151, v151\n"
    "s_waitcnt vmcnt(8)\n"
    "v_smfmac_f32_32x32x32_bf16 v[192:207], v[72:75], v[128:135], v16\n"
    "buffer_load_dwordx4 v[100:103], v4, s[40:43], s52 offen offset:1024 sc1\n"
    "v_exp_f32 v152, v152\n"
    "v_exp_f32 v153, v153\n"
    "v_exp_f32 v154, v154\n"
    "v_exp_f32 v155, v155\n"
    "s_waitcnt vmcnt(8)\n"
    "v_smfmac_f32_32x32x32_bf16 v[192:207], v[76:79], v[136:143], v16\n"
    "buffer_load_dwordx4 v[104:107], v5, s[40:43], s52 offen offset:2048 sc1\n"
    "s_waitcnt vmcnt(7)\n"
    "v_and_b32 v56, v14, v56\n"
    "v_and_b32 v57, v14, v57\n"
    "v_and_b32 v62, v15, v62\n"
    "v_and_b32 v63, v15, v63\n"
    "v_exp_f32 v156, v156\n"
    "v_exp_f32 v157, v157\n"
    "v_exp_f32 v158, v158\n"
    "v_exp_f32 v159, v159\n"
    "v_mfma_f32_32x32x16_bf16 v[176:191], v[64:67], v[56:59], 0\n"
    "v_exp_f32 v160, v160\n"
    "v_exp_f32 v161, v161\n"
    "v_exp_f32 v162, v162\n"
    "v_exp_f32 v163, v163\n"
    "v_mfma_f32_32x32x16_bf16 v[176:191], v[68:71], v[60:63], v[176:191]\n"
    "buffer_load_dwordx4 v[108:111], v5, s[40:43], s52 offen offset:3072 sc1\n"
    "v_exp_f32 v164, v164\n"
    "v_exp_f32 v165, v165\n"
    "v_exp_f32 v166, v166\n"
    "v_exp_f32 v167, v167\n"
    "v_smfmac_f32_32x32x32_bf16 v[208:223], v[72:75], v[144:151], v16\n"
    "s_add_u32 s52, s52, 61440\n"
    "v_exp_f32 v168, v168\n"
    "v_exp_f32 v169, v169\n"
    "v_exp_f32 v170, v170\n"
    "v_exp_f32 v171, v171\n"
    "v_smfmac_f32_32x32x32_bf16 v[208:223], v[76:79], v[152:159], v16\n"
    "buffer_load_dwordx4 v[112:115], v4, s[40:43], s52 offen offset:0 sc1\n"
    "v_exp_f32 v172, v172\n"
    "v_exp_f32 v173, v173\n"
    "v_exp_f32 v174, v174\n"
    "v_exp_f32 v175, v175\n"
    "s_waitcnt vmcnt(8)\n"
    "v_mfma_f32_32x32x16_bf16 v[128:143], v[80:83], v[32:35], 0\n"
    "v_exp_f32 v176, v176\n"
    "v_exp_f32 v177, v177\n"
    "v_exp_f32 v178, v178\n"
    "v_exp_f32 v179, v179\n"
    "s_waitcnt vmcnt(7)\n"
    "v_mfma_f32_32x32x16_bf16 v[128:143], v[84:87], v[36:39], v[128:143]\n"
    "v_exp_f32 v180, v180\n"
    "v_exp_f32 v181, v181\n"
    "v_exp_f32 v182, v182\n"
    "v_exp_f32 v183, v183\n"
    "v_smfmac_f32_32x32x32_bf16 v[224:239], v[72:75], v[160:167], v16\n"
    "buffer_load_dwordx4 v[116:119], v4, s[40:43], s52 offen offset:1024 sc1\n"
    "v_exp_f32 v184, v184\n"
    "v_exp_f32 v185, v185\n"
    "v_exp_f32 v186, v186\n"
    "v_exp_f32 v187, v187\n"
    "v_smfmac_f32_32x32x32_bf16 v[224:239], v[76:79], v[168:175], v16\n"
    "buffer_load_dwordx4 v[120:123], v5, s[40:43], s52 offen offset:2048 sc1\n"
    "v_exp_f32 v188, v188\n"
    "v_exp_f32 v189, v189\n"
    "v_exp_f32 v190, v190\n"
    "v_exp_f32 v191, v191\n"
    "v_mfma_f32_32x32x16_bf16 v[144:159], v[80:83], v[40:43], 0\n"
    "v_exp_f32 v128, v128\n"
    "v_exp_f32 v129, v129\n"
    "v_exp_f32 v130, v130\n"
    "v_exp_f32 v131, v131\n"
    "v_mfma_f32_32x32x16_bf16 v[144:159], v[84:87], v[44:47], v[144:159]\n"
    "v_exp_f32 v132, v132\n"
    "v_exp_f32 v133, v133\n"
    "v_exp_f32 v134, v134\n"
    "v_exp_f32 v135, v135\n"
    "v_smfmac_f32_32x32x32_bf16 v[240:255], v[72:75], v[176:183], v16\n"
    "buffer_load_dwordx4 v[124:127], v5, s[40:43], s52 offen offset:3072 sc1\n"
    "v_exp_f32 v136, v136\n"
    "v_exp_f32 v137, v137\n"
    "v_exp_f32 v138, v138\n"
    "v_exp_f32 v139, v139\n"
    "v_smfmac_f32_32x32x32_bf16 v[240:255], v[76:79], v[184:191], v16\n"
    "s_add_u32 s52, s52, 61440\n"
    "v_exp_f32 v140, v140\n"
    "v_exp_f32 v141, v141\n"
    "v_exp_f32 v142, v142\n"
    "v_exp_f32 v143, v143\n"
    "v_mfma_f32_32x32x16_bf16 v[160:175], v[80:83], v[48:51], 0\n"
    "v_exp_f32 v144, v144\n"
    "v_exp_f32 v145, v145\n"
    "v_exp_f32 v146, v146\n"
    "v_exp_f32 v147, v147\n"
    "v_mfma_f32_32x32x16_bf16 v[160:175], v[84:87], v[52:55], v[160:175]\n"
    "s_waitcnt vmcnt(8)\n"
    "v_exp_f32 v148, v148\n"
    "v_exp_f32 v149, v149\n"
    "v_exp_f32 v150, v150\n"
    "v_exp_f32 v151, v151\n"
    "v_smfmac_f32_32x32x32_bf16 v[192:207], v[88:91], v[128:135], v16\n"
    "buffer_load_dwordx4 v[64:67], v4, s[40:43], s52 offen offset:0 sc1\n"
    "v_exp_f32 v152, v152\n"
    "v_exp_f32 v153, v153\n"
    "v_exp_f32 v154, v154\n"
    "v_exp_f32 v155, v155\n"
    "v_smfmac_f32_32x32x32_bf16 v[192:207], v[92:95], v[136:143], v16\n"
    "buffer_load_dwordx4 v[68:71], v4, s[40:43], s52 offen offset:1024 sc1\n"
    "v_exp_f32 v156, v156\n"
    "v_exp_f32 v157, v157\n"
    "v_exp_f32 v158, v158\n"
    "v_exp_f32 v159, v159\n"
    "v_mfma_f32_32x32x16_bf16 v[176:191], v[80:83], v[56:59], 0\n"
    "v_exp_f32 v160, v160\n"
    "v_exp_f32 v161, v161\n"
    "v_exp_f32 v162, v162\n"
    "v_exp_f32 v163, v163\n"
    "v_mfma_f32_32x32x16_bf16 v[176:191], v[84:87], v[60:63], v[176:191]\n"
    "v_exp_f32 v164, v164\n"
    "v_exp_f32 v165, v165\n"
    "v_exp_f32 v166, v166\n"
    "v_exp_f32 v167, v167\n"
    "v_smfmac_f32_32x32x32_bf16 v[208:223], v[88:91], v[144:151], v16\n"
    "buffer_load_dwordx4 v[72:75], v5, s[40:43], s52 offen offset:2048 sc1\n"
    "v_exp_f32 v168, v168\n"
    "v_exp_f32 v169, v169\n"
    "v_exp_f32 v170, v170\n"
    "v_exp_f32 v171, v171\n"
    "v_smfmac_f32_32x32x32_bf16 v[208:223], v[92:95], v[152:159], v16\n"
    "buffer_load_dwordx4 v[76:79], v5, s[40:43], s52 offen offset:3072 sc1\n"
    "v_exp_f32 v172, v172\n"
    "v_exp_f32 v173, v173\n"
    "v_exp_f32 v174, v174\n"
    "v_exp_f32 v175, v175\n"
    "s_waitcnt vmcnt(11)\n"
    "v_mfma_f32_32x32x16_bf16 v[128:143], v[96:99], v[32:35], 0\n"
    "s_add_u32 s52, s52, 61440\n"
    "v_exp_f32 v176, v176\n"
    "v_exp_f32 v177, v177\n"
    "v_exp_f32 v178, v178\n"
    "v_exp_f32 v179, v179\n"
    "s_waitcnt vmcnt(10)\n"
    "v_mfma_f32_32x32x16_bf16 v[128:143], v[100:103], v[36:39], v[128:143]\n"
    "v_exp_f32 v180, v180\n"
    "v_exp_f32 v181, v181\n"
    "v_exp_f32 v182, v182\n"
    "v_exp_f32 v183, v183\n"
    "v_smfmac_f32_32x32x32_bf16 v[224:239], v[88:91], v[160:167], v16\n"
    "buffer_load_dwordx4 v[80:83], v4, s[40:43], s52 offen offset:0 sc1\n"
    "v_exp_f32 v184, v184\n"
    "v_exp_f32 v185, v185\n"
    "v_exp_f32 v186, v186\n"
    "v_exp_f32 v187, v187\n"
    "v_smfmac_f32_32x32x32_bf16 v[224:239], v[92:95], v[168:175], v16\n"
    "v_exp_f32 v188, v188\n"
    "v_exp_f32 v189, v189\n"
    "v_exp_f32 v190, v190\n"
    "v_exp_f32 v191, v191\n"
    "v_mfma_f32_32x32x16_bf16 v[144:159], v[96:99], v[40:43], 0\n"
    "v_exp_f32 v128, v128\n"
    "v_exp_f32 v129, v129\n"
    "v_exp_f32 v130, v130\n"
    "v_exp_f32 v131, v131\n"
    "v_mfma_f32_32x32x16_bf16 v[144:159], v[100:103], v[44:47], v[144:159]\n"
    "v_exp_f32 v132, v132\n"
    "v_exp_f32 v133, v133\n"
    "v_exp_f32 v134, v134\n"
    "v_exp_f32 v135, v135\n"
    "v_smfmac_f32_32x32x32_bf16 v[240:255], v[88:91], v[176:183], v16\n"
    "buffer_load_dwordx4 v[84:87], v4, s[40:43], s52 offen offset:1024 sc1\n"
    "v_exp_f32 v136, v136\n"
    "v_exp_f32 v137, v137\n"
    "v_exp_f32 v138, v138\n"
    "v_exp_f32 v139, v139\n"
    "v_smfmac_f32_32x32x32_bf16 v[240:255], v[92:95], v[184:191], v16\n"
    "v_exp_f32 v140, v140\n"
    "v_exp_f32 v141, v141\n"
    "v_exp_f32 v142, v142\n"
    "v_exp_f32 v143, v143\n"
    "v_mfma_f32_32x32x16_bf16 v[160:175], v[96:99], v[48:51], 0\n"
    "v_exp_f32 v144, v144\n"
    "v_exp_f32 v145, v145\n"
    "v_exp_f32 v146, v146\n"
    "v_exp_f32 v147, v147\n"
    "v_mfma_f32_32x32x16_bf16 v[160:175], v[100:103], v[52:55], v[160:175]\n"
    "s_waitcnt vmcnt(10)\n"
    "v_exp_f32 v148, v148\n"
    "v_exp_f32 v149, v149\n"
    "v_exp_f32 v150, v150\n"
    "v_exp_f32 v151, v151\n"
    "v_smfmac_f32_32x32x32_bf16 v[192:207], v[104:107], v[128:135], v16\n"
    "buffer_load_dwordx4 v[88:91], v5, s[40:43], s52 offen offset:2048 sc1\n"
    "v_exp_f32 v152, v152\n"
    "v_exp_f32 v153, v153\n"
    "v_exp_f32 v154, v154\n"
    "v_exp_f32 v155, v155\n"
    "v_smfmac_f32_32x32x32_bf16 v[192:207], v[108:111], v[136:143], v16\n"
    "v_exp_f32 v156, v156\n"
    "v_exp_f32 v157, v157\n"
    "v_exp_f32 v158, v158\n"
    "v_exp_f32 v159, v159\n"
    "v_mfma_f32_32x32x16_bf16 v[176:191], v[96:99], v[56:59], 0\n"
    "v_exp_f32 v160, v160\n"
    "v_exp_f32 v161, v161\n"
    "v_exp_f32 v162, v162\n"
    "v_exp_f32 v163, v163\n"
    "v_mfma_f32_32x32x16_bf16 v[176:191], v[100:103], v[60:63], v[176:191]\n"
    "v_exp_f32 v164, v164\n"
    "v_exp_f32 v165, v165\n"
    "v_exp_f32 v166, v166\n"
    "v_exp_f32 v167, v167\n"
    "v_smfmac_f32_32x32x32_bf16 v[208:223], v[104:107], v[144:151], v16\n"
    "buffer_load_dwordx4 v[92:95], v5, s[40:43], s52 offen offset:3072 sc1\n"
    "v_exp_f32 v168, v168\n"
    "v_exp_f32 v169, v169\n"
    "v_exp_f32 v170, v170\n"
    "v_exp_f32 v171, v171\n"
    "v_smfmac_f32_32x32x32_bf16 v[208:223], v[108:111], v[152:159], v16\n"
    "s_add_u32 s52, s52, 61440\n"
    "v_exp_f32 v172, v172\n"
    "v_exp_f32 v173, v173\n"
    "v_exp_f32 v174, v174\n"
    "v_exp_f32 v175, v175\n"
    "s_waitcnt vmcnt(11)\n"
    "v_mfma_f32_32x32x16_bf16 v[128:143], v[112:115], v[32:35], 0\n"
    "v_exp_f32 v176, v176\n"
    "v_exp_f32 v177, v177\n"
    "v_exp_f32 v178, v178\n"
    "v_exp_f32 v179, v179\n"
    "s_waitcnt vmcnt(10)\n"
    "v_mfma_f32_32x32x16_bf16 v[128:143], v[116:119], v[36:39], v[128:143]\n"
    "v_exp_f32 v180, v180\n"
    "v_exp_f32 v181, v181\n"
    "v_exp_f32 v182, v182\n"
    "v_exp_f32 v183, v183\n"
    "v_smfmac_f32_32x32x32_bf16 v[224:239], v[104:107], v[160:167], v16\n"
    "buffer_load_dwordx4 v[96:99], v4, s[40:43], s52 offen offset:0 sc1\n"
    "v_exp_f32 v184, v184\n"
    "v_exp_f32 v185, v185\n"
    "v_exp_f32 v186, v186\n"
    "v_exp_f32 v187, v187\n"
    "v_smfmac_f32_32x32x32_bf16 v[224:239], v[108:111], v[168:175], v16\n"
    "v_exp_f32 v188, v188\n"
    "v_exp_f32 v189, v189\n"
    "v_exp_f32 v190, v190\n"
    "v_exp_f32 v191, v191\n"
    "v_mfma_f32_32x32x16_bf16 v[144:159], v[112:115], v[40:43], 0\n"
    "v_exp_f32 v128, v128\n"
    "v_exp_f32 v129, v129\n"
    "v_exp_f32 v130, v130\n"
    "v_exp_f32 v131, v131\n"
    "v_mfma_f32_32x32x16_bf16 v[144:159], v[116:119], v[44:47], v[144:159]\n"
    "v_exp_f32 v132, v132\n"
    "v_exp_f32 v133, v133\n"
    "v_exp_f32 v134, v134\n"
    "v_exp_f32 v135, v135\n"
    "v_smfmac_f32_32x32x32_bf16 v[240:255], v[104:107], v[176:183], v16\n"
    "buffer_load_dwordx4 v[100:103], v4, s[40:43], s52 offen offset:1024 sc1\n"
    "v_exp_f32 v136, v136\n"
    "v_exp_f32 v137, v137\n"
    "v_exp_f32 v138, v138\n"
    "v_exp_f32 v139, v139\n"
    "v_smfmac_f32_32x32x32_bf16 v[240:255], v[108:111], v[184:191], v16\n"
    "v_exp_f32 v140, v140\n"
    "v_exp_f32 v141, v141\n"
    "v_exp_f32 v142, v142\n"
    "v_exp_f32 v143, v143\n"
    "v_mfma_f32_32x32x16_bf16 v[160:175], v[112:115], v[48:51], 0\n"
    "v_exp_f32 v144, v144\n"
    "v_exp_f32 v145, v145\n"
    "v_exp_f32 v146, v146\n"
    "v_exp_f32 v147, v147\n"
    "v_mfma_f32_32x32x16_bf16 v[160:175], v[116:119], v[52:55], v[160:175]\n"
    "s_waitcnt vmcnt(10)\n"
    "v_exp_f32 v148, v148\n"
    "v_exp_f32 v149, v149\n"
    "v_exp_f32 v150, v150\n"
    "v_exp_f32 v151, v151\n"
    "v_smfmac_f32_32x32x32_bf16 v[192:207], v[120:123], v[128:135], v16\n"
    "buffer_load_dwordx4 v[104:107], v5, s[40:43], s52 offen offset:2048 sc1\n"
    "v_exp_f32 v152, v152\n"
    "v_exp_f32 v153, v153\n"
    "v_exp_f32 v154, v154\n"
    "v_exp_f32 v155, v155\n"
    "v_smfmac_f32_32x32x32_bf16 v[192:207], v[124:127], v[136:143], v16\n"
    "v_exp_f32 v156, v156\n"
    "v_exp_f32 v157, v157\n"
    "v_exp_f32 v158, v158\n"
    "v_exp_f32 v159, v159\n"
    "v_mfma_f32_32x32x16_bf16 v[176:191], v[112:115], v[56:59], 0\n"
    "v_exp_f32 v160, v160\n"
    "v_exp_f32 v161, v161\n"
    "v_exp_f32 v162, v162\n"
    "v_exp_f32 v163, v163\n"
    "v_mfma_f32_32x32x16_bf16 v[176:191], v[116:119], v[60:63], v[176:191]\n"
    "v_exp_f32 v164, v164\n"
    "v_exp_f32 v165, v165\n"
    "v_exp_f32 v166, v166\n"
    "v_exp_f32 v167, v167\n"
    "v_smfmac_f32_32x32x32_bf16 v[208:223], v[120:123], v[144:151], v16\n"
    "buffer_load_dwordx4 v[108:111], v5, s[40:43], s52 offen offset:3072 sc1\n"
    "v_exp_f32 v168, v168\n"
    "v_exp_f32 v169, v169\n"
    "v_exp_f32 v170, v170\n"
    "v_exp_f32 v171, v171\n"
    "v_smfmac_f32_32x32x32_bf16 v[208:223], v[124:127], v[152:159], v16\n"
    "s_add_u32 s52, s52, 61440\n"
    "v_exp_f32 v172, v172\n"
    "v_exp_f32 v173, v173\n"
    "v_exp_f32 v174, v174\n"
    "v_exp_f32 v175, v175\n"
    "s_waitcnt vmcnt(11)\n"
    "v_mfma_f32_32x32x16_bf16 v[128:143], v[64:67], v[32:35], 0\n"
    "v_exp_f32 v176, v176\n"
    "v_exp_f32 v177, v177\n"
    "v_exp_f32 v178, v178\n"
    "v_exp_f32 v179, v179\n"
    "s_waitcnt vmcnt(10)\n"
    "v_mfma_f32_32x32x16_bf16 v[128:143], v[68:71], v[36:39], v[128:143]\n"
    "v_exp_f32 v180, v180\n"
    "v_exp_f32 v181, v181\n"
    "v_exp_f32 v182, v182\n"
    "v_exp_f32 v183, v183\n"
    "v_smfmac_f32_32x32x32_bf16 v[224:239], v[120:123], v[160:167], v16\n"
    "buffer_load_dwordx4 v[112:115], v4, s[40:43], s52 offen offset:0 sc1\n"
    "v_exp_f32 v184, v184\n"
    "v_exp_f32 v185, v185\n"
    "v_exp_f32 v186, v186\n"
    "v_exp_f32 v187, v187\n"
    "v_smfmac_f32_32x32x32_bf16 v[224:239], v[124:127], v[168:175], v16\n"
    "v_exp_f32 v188, v188\n"
    "v_exp_f32 v189, v189\n"
    "v_exp_f32 v190, v190\n"
    "v_exp_f32 v191, v191\n"
    "v_mfma_f32_32x32x16_bf16 v[144:159], v[64:67], v[40:43], 0\n"
    "v_exp_f32 v128, v128\n"
    "v_exp_f32 v129, v129\n"
    "v_exp_f32 v130, v130\n"
    "v_exp_f32 v131, v131\n"
    "v_mfma_f32_32x32x16_bf16 v[144:159], v[68:71], v[44:47], v[144:159]\n"
    "v_exp_f32 v132, v132\n"
    "v_exp_f32 v133, v133\n"
    "v_exp_f32 v134, v134\n"
    "v_exp_f32 v135, v135\n"
    "v_smfmac_f32_32x32x32_bf16 v[240:255], v[120:123], v[176:183], v16\n"
    "buffer_load_dwordx4 v[116:119], v4, s[40:43], s52 offen offset:1024 sc1\n"
    "v_exp_f32 v136, v136\n"
    "v_exp_f32 v137, v137\n"
    "v_exp_f32 v138, v138\n"
    "v_exp_f32 v139, v139\n"
    "v_smfmac_f32_32x32x32_bf16 v[240:255], v[124:127], v[184:191], v16\n"
    "v_exp_f32 v140, v140\n"
    "v_exp_f32 v141, v141\n"
    "v_exp_f32 v142, v142\n"
    "v_exp_f32 v143, v143\n"
    "v_mfma_f32_32x32x16_bf16 v[160:175], v[64:67], v[48:51], 0\n"
    "v_exp_f32 v144, v144\n"
    "v_exp_f32 v145, v145\n"
    "v_exp_f32 v146, v146\n"
    "v_exp_f32 v147, v147\n"
    "v_mfma_f32_32x32x16_bf16 v[160:175], v[68:71], v[52:55], v[160:175]\n"
    "s_waitcnt vmcnt(10)\n"
    "v_exp_f32 v148, v148\n"
    "v_exp_f32 v149, v149\n"
    "v_exp_f32 v150, v150\n"
    "v_exp_f32 v151, v151\n"
    "v_smfmac_f32_32x32x32_bf16 v[192:207], v[72:75], v[128:135], v16\n"
    "buffer_load_dwordx4 v[120:123], v5, s[40:43], s52 offen offset:2048 sc1\n"
    "v_exp_f32 v152, v152\n"
    "v_exp_f32 v153, v153\n"
    "v_exp_f32 v154, v154\n"
    "v_exp_f32 v155, v155\n"
    "v_smfmac_f32_32x32x32_bf16 v[192:207], v[76:79], v[136:143], v16\n"
    "v_exp_f32 v156, v156\n"
    "v_exp_f32 v157, v157\n"
    "v_exp_f32 v158, v158\n"
    "v_exp_f32 v159, v159\n"
    "v_mfma_f32_32x32x16_bf16 v[176:191], v[64:67], v[56:59], 0\n"
    "v_exp_f32 v160, v160\n"
    "v_exp_f32 v161, v161\n"
    "v_exp_f32 v162, v162\n"
    "v_exp_f32 v163, v163\n"
    "v_mfma_f32_32x32x16_bf16 v[176:191], v[68:71], v[60:63], v[176:191]\n"
    "v_exp_f32 v164, v164\n"
    "v_exp_f32 v165, v165\n"
    "v_exp_f32 v166, v166\n"
    "v_exp_f32 v167, v167\n"
    "v_smfmac_f32_32x32x32_bf16 v[208:223], v[72:75], v[144:151], v16\n"
    "buffer_load_dwordx4 v[124:127], v5, s[40:43], s52 offen offset:3072 sc1\n"
    "v_exp_f32 v168, v168\n"
    "v_exp_f32 v169, v169\n"
    "v_exp_f32 v170, v170\n"
    "v_exp_f32 v171, v171\n"
    "v_smfmac_f32_32x32x32_bf16 v[208:223], v[76:79], v[152:159], v16\n"
    "s_add_u32 s52, s52, 61440\n"
    "v_exp_f32 v172, v172\n"
    "v_exp_f32 v173, v173\n"
    "v_exp_f32 v174, v174\n"
    "v_exp_f32 v175, v175\n"
    "s_waitcnt vmcnt(11)\n"
    "v_mfma_f32_32x32x16_bf16 v[128:143], v[80:83], v[32:35], 0\n"
    "v_exp_f32 v176, v176\n"
    "v_exp_f32 v177, v177\n"
    "v_exp_f32 v178, v178\n"
    "v_exp_f32 v179, v179\n"
    "s_waitcnt vmcnt(10)\n"
    "v_mfma_f32_32x32x16_bf16 v[128:143], v[84:87], v[36:39], v[128:143]\n"
    "v_exp_f32 v180, v180\n"
    "v_exp_f32 v181, v181\n"
    "v_exp_f32 v182, v182\n"
    "v_exp_f32 v183, v183\n"
    "v_smfmac_f32_32x32x32_bf16 v[224:239], v[72:75], v[160:167], v16\n"
    "buffer_load_dwordx4 v[64:67], v4, s[40:43], s52 offen offset:0 sc1\n"
    "v_exp_f32 v184, v184\n"
    "v_exp_f32 v185, v185\n"
    "v_exp_f32 v186, v186\n"
    "v_exp_f32 v187, v187\n"
    "v_smfmac_f32_32x32x32_bf16 v[224:239], v[76:79], v[168:175], v16\n"
    "v_exp_f32 v188, v188\n"
    "v_exp_f32 v189, v189\n"
    "v_exp_f32 v190, v190\n"
    "v_exp_f32 v191, v191\n"
    "v_mfma_f32_32x32x16_bf16 v[144:159], v[80:83], v[40:43], 0\n"
    "v_exp_f32 v128, v128\n"
    "v_exp_f32 v129, v129\n"
    "v_exp_f32 v130, v130\n"
    "v_exp_f32 v131, v131\n"
    "v_mfma_f32_32x32x16_bf16 v[144:159], v[84:87], v[44:47], v[144:159]\n"
    "v_exp_f32 v132, v132\n"
    "v_exp_f32 v133, v133\n"
    "v_exp_f32 v134, v134\n"
    "v_exp_f32 v135, v135\n"
    "v_smfmac_f32_32x32x32_bf16 v[240:255], v[72:75], v[176:183], v16\n"
    "buffer_load_dwordx4 v[68:71], v4, s[40:43], s52 offen offset:1024 sc1\n"
    "v_exp_f32 v136, v136\n"
    "v_exp_f32 v137, v137\n"
    "v_exp_f32 v138, v138\n"
    "v_exp_f32 v139, v139\n"
    "v_smfmac_f32_32x32x32_bf16 v[240:255], v[76:79], v[184:191], v16\n"
    "v_exp_f32 v140, v140\n"
    "v_exp_f32 v141, v141\n"
    "v_exp_f32 v142, v142\n"
    "v_exp_f32 v143, v143\n"
    "v_mfma_f32_32x32x16_bf16 v[160:175], v[80:83], v[48:51], 0\n"
    "v_exp_f32 v144, v144\n"
    "v_exp_f32 v145, v145\n"
    "v_exp_f32 v146, v146\n"
    "v_exp_f32 v147, v147\n"
    "v_mfma_f32_32x32x16_bf16 v[160:175], v[84:87], v[52:55], v[160:175]\n"
    "s_waitcnt vmcnt(10)\n"
    "v_exp_f32 v148, v148\n"
    "v_exp_f32 v149, v149\n"
    "v_exp_f32 v150, v150\n"
    "v_exp_f32 v151, v151\n"
    "v_smfmac_f32_32x32x32_bf16 v[192:207], v[88:91], v[128:135], v16\n"
    "buffer_load_dwordx4 v[72:75], v5, s[40:43], s52 offen offset:2048 sc1\n"
    "v_exp_f32 v152, v152\n"
    "v_exp_f32 v153, v153\n"
    "v_exp_f32 v154, v154\n"
    "v_exp_f32 v155, v155\n"
    "v_smfmac_f32_32x32x32_bf16 v[192:207], v[92:95], v[136:143], v16\n"
    "v_exp_f32 v156, v156\n"
    "v_exp_f32 v157, v157\n"
    "v_exp_f32 v158, v158\n"
    "v_exp_f32 v159, v159\n"
    "v_mfma_f32_32x32x16_bf16 v[176:191], v[80:83], v[56:59], 0\n"
    "v_exp_f32 v160, v160\n"
    "v_exp_f32 v161, v161\n"
    "v_exp_f32 v162, v162\n"
    "v_exp_f32 v163, v163\n"
    "v_mfma_f32_32x32x16_bf16 v[176:191], v[84:87], v[60:63], v[176:191]\n"
    "v_exp_f32 v164, v164\n"
    "v_exp_f32 v165, v165\n"
    "v_exp_f32 v166, v166\n"
    "v_exp_f32 v167, v167\n"
    "v_smfmac_f32_32x32x32_bf16 v[208:223], v[88:91], v[144:151], v16\n"
    "buffer_load_dwordx4 v[76:79], v5, s[40:43], s52 offen offset:3072 sc1\n"
    "v_exp_f32 v168, v168\n"
    "v_exp_f32 v169, v169\n"
    "v_exp_f32 v170, v170\n"
    "v_exp_f32 v171, v171\n"
    "v_smfmac_f32_32x32x32_bf16 v[208:223], v[92:95], v[152:159], v16\n"
    "s_add_u32 s52, s52, 61440\n"
    "v_exp_f32 v172, v172\n"
    "v_exp_f32 v173, v173\n"
    "v_exp_f32 v174, v174\n"
    "v_exp_f32 v175, v175\n"
    "s_waitcnt vmcnt(11)\n"
    "v_mfma_f32_32x32x16_bf16 v[128:143], v[96:99], v[32:35], 0\n"
    "v_exp_f32 v176, v176\n"
    "v_exp_f32 v177, v177\n"
    "v_exp_f32 v178, v178\n"
    "v_exp_f32 v179, v179\n"
    "s_waitcnt vmcnt(10)\n"
    "v_mfma_f32_32x32x16_bf16 v[128:143], v[100:103], v[36:39], v[128:143]\n"
    "v_exp_f32 v180, v180\n"
    "v_exp_f32 v181, v181\n"
    "v_exp_f32 v182, v182\n"
    "v_exp_f32 v183, v183\n"
    "v_smfmac_f32_32x32x32_bf16 v[224:239], v[88:91], v[160:167], v16\n"
    "buffer_load_dwordx4 v[80:83], v4, s[40:43], s52 offen offset:0 sc1\n"
    "v_exp_f32 v184, v184\n"
    "v_exp_f32 v185, v185\n"
    "v_exp_f32 v186, v186\n"
    "v_exp_f32 v187, v187\n"
    "v_smfmac_f32_32x32x32_bf16 v[224:239], v[92:95], v[168:175], v16\n"
    "v_exp_f32 v188, v188\n"
    "v_exp_f32 v189, v189\n"
    "v_exp_f32 v190, v190\n"
    "v_exp_f32 v191, v191\n"
    "v_mfma_f32_32x32x16_bf16 v[144:159], v[96:99], v[40:43], 0\n"
    "v_exp_f32 v128, v128\n"
    "v_exp_f32 v129, v129\n"
    "v_exp_f32 v130, v130\n"
    "v_exp_f32 v131, v131\n"
    "v_mfma_f32_32x32x16_bf16 v[144:159], v[100:103], v[44:47], v[144:159]\n"
    "v_exp_f32 v132, v132\n"
    "v_exp_f32 v133, v133\n"
    "v_exp_f32 v134, v134\n"
    "v_exp_f32 v135, v135\n"
    "v_smfmac_f32_32x32x32_bf16 v[240:255], v[88:91], v[176:183], v16\n"
    "buffer_load_dwordx4 v[84:87], v4, s[40:43], s52 offen offset:1024 sc1\n"
    "v_exp_f32 v136, v136\n"
    "v_exp_f32 v137, v137\n"
    "v_exp_f32 v138, v138\n"
    "v_exp_f32 v139, v139\n"
    "v_smfmac_f32_32x32x32_bf16 v[240:255], v[92:95], v[184:191], v16\n"
    "v_exp_f32 v140, v140\n"
    "v_exp_f32 v141, v141\n"
    "v_exp_f32 v142, v142\n"
    "v_exp_f32 v143, v143\n"
    "v_mfma_f32_32x32x16_bf16 v[160:175], v[96:99], v[48:51], 0\n"
    "v_exp_f32 v144, v144\n"
    "v_exp_f32 v145, v145\n"
    "v_exp_f32 v146, v146\n"
    "v_exp_f32 v147, v147\n"
    "v_mfma_f32_32x32x16_bf16 v[160:175], v[100:103], v[52:55], v[160:175]\n"
    "s_waitcnt vmcnt(10)\n"
    "v_exp_f32 v148, v148\n"
    "v_exp_f32 v149, v149\n"
    "v_exp_f32 v150, v150\n"
    "v_exp_f32 v151, v151\n"
    "v_smfmac_f32_32x32x32_bf16 v[192:207], v[104:107], v[128:135], v16\n"
    "buffer_load_dwordx4 v[88:91], v5, s[40:43], s52 offen offset:2048 sc1\n"
    "v_exp_f32 v152, v152\n"
    "v_exp_f32 v153, v153\n"
    "v_exp_f32 v154, v154\n"
    "v_exp_f32 v155, v155\n"
    "v_smfmac_f32_32x32x32_bf16 v[192:207], v[108:111], v[136:143], v16\n"
    "v_exp_f32 v156, v156\n"
    "v_exp_f32 v157, v157\n"
    "v_exp_f32 v158, v158\n"
    "v_exp_f32 v159, v159\n"
    "v_mfma_f32_32x32x16_bf16 v[176:191], v[96:99], v[56:59], 0\n"
    "v_exp_f32 v160, v160\n"
    "v_exp_f32 v161, v161\n"
    "v_exp_f32 v162, v162\n"
    "v_exp_f32 v163, v163\n"
    "v_mfma_f32_32x32x16_bf16 v[176:191], v[100:103], v[60:63], v[176:191]\n"
    "v_exp_f32 v164, v164\n"
    "v_exp_f32 v165, v165\n"
    "v_exp_f32 v166, v166\n"
    "v_exp_f32 v167, v167\n"
    "v_smfmac_f32_32x32x32_bf16 v[208:223], v[104:107], v[144:151], v16\n"
    "buffer_load_dwordx4 v[92:95], v5, s[40:43], s52 offen offset:3072 sc1\n"
    "v_exp_f32 v168, v168\n"
    "v_exp_f32 v169, v169\n"
    "v_exp_f32 v170, v170\n"
    "v_exp_f32 v171, v171\n"
    "v_smfmac_f32_32x32x32_bf16 v[208:223], v[108:111], v[152:159], v16\n"
    "s_add_u32 s52, s52, 61440\n"
    "v_exp_f32 v172, v172\n"
    "v_exp_f32 v173, v173\n"
    "v_exp_f32 v174, v174\n"
    "v_exp_f32 v175, v175\n"
    "s_waitcnt vmcnt(11)\n"
    "v_mfma_f32_32x32x16_bf16 v[128:143], v[112:115], v[32:35], 0\n"
    "v_exp_f32 v176, v176\n"
    "v_exp_f32 v177, v177\n"
    "v_exp_f32 v178, v178\n"
    "v_exp_f32 v179, v179\n"
    "s_waitcnt vmcnt(10)\n"
    "v_mfma_f32_32x32x16_bf16 v[128:143], v[116:119], v[36:39], v[128:143]\n"
    "v_exp_f32 v180, v180\n"
    "v_exp_f32 v181, v181\n"
    "v_exp_f32 v182, v182\n"
    "v_exp_f32 v183, v183\n"
    "v_smfmac_f32_32x32x32_bf16 v[224:239], v[104:107], v[160:167], v16\n"
    "buffer_load_dwordx4 v[96:99], v4, s[40:43], s52 offen offset:0 sc1\n"
    "v_exp_f32 v184, v184\n"
    "v_exp_f32 v185, v185\n"
    "v_exp_f32 v186, v186\n"
    "v_exp_f32 v187, v187\n"
    "v_smfmac_f32_32x32x32_bf16 v[224:239], v[108:111], v[168:175], v16\n"
    "v_exp_f32 v188, v188\n"
    "v_exp_f32 v189, v189\n"
    "v_exp_f32 v190, v190\n"
    "v_exp_f32 v191, v191\n"
    "v_mfma_f32_32x32x16_bf16 v[144:159], v[112:115], v[40:43], 0\n"
    "v_exp_f32 v128, v128\n"
    "v_exp_f32 v129, v129\n"
    "v_exp_f32 v130, v130\n"
    "v_exp_f32 v131, v131\n"
    "v_mfma_f32_32x32x16_bf16 v[144:159], v[116:119], v[44:47], v[144:159]\n"
    "v_exp_f32 v132, v132\n"
    "v_exp_f32 v133, v133\n"
    "v_exp_f32 v134, v134\n"
    "v_exp_f32 v135, v135\n"
    "v_smfmac_f32_32x32x32_bf16 v[240:255], v[104:107], v[176:183], v16\n"
    "buffer_load_dwordx4 v[100:103], v4, s[40:43], s52 offen offset:1024 sc1\n"
    "v_exp_f32 v136, v136\n"
    "v_exp_f32 v137, v137\n"
    "v_exp_f32 v138, v138\n"
    "v_exp_f32 v139, v139\n"
    "v_smfmac_f32_32x32x32_bf16 v[240:255], v[108:111], v[184:191], v16\n"
    "v_exp_f32 v140, v140\n"
    "v_exp_f32 v141, v141\n"
    "v_exp_f32 v142, v142\n"
    "v_exp_f32 v143, v143\n"
    "v_mfma_f32_32x32x16_bf16 v[160:175], v[112:115], v[48:51], 0\n"
    "v_exp_f32 v144, v144\n"
    "v_exp_f32 v145, v145\n"
    "v_exp_f32 v146, v146\n"
    "v_exp_f32 v147, v147\n"
    "v_mfma_f32_32x32x16_bf16 v[160:175], v[116:119], v[52:55], v[160:175]\n"
    "s_waitcnt vmcnt(10)\n"
    "v_exp_f32 v148, v148\n"
    "v_exp_f32 v149, v149\n"
    "v_exp_f32 v150, v150\n"
    "v_exp_f32 v151, v151\n"
    "v_smfmac_f32_32x32x32_bf16 v[192:207], v[120:123], v[128:135], v16\n"
    "buffer_load_dwordx4 v[104:107], v5, s[40:43], s52 offen offset:2048 sc1\n"
    "v_exp_f32 v152, v152\n"
    "v_exp_f32 v153, v153\n"
    "v_exp_f32 v154, v154\n"
    "v_exp_f32 v155, v155\n"
    "v_smfmac_f32_32x32x32_bf16 v[192:207], v[124:127], v[136:143], v16\n"
    "v_exp_f32 v156, v156\n"
    "v_exp_f32 v157, v157\n"
    "v_exp_f32 v158, v158\n"
    "v_exp_f32 v159, v159\n"
    "v_mfma_f32_32x32x16_bf16 v[176:191], v[112:115], v[56:59], 0\n"
    "v_exp_f32 v160, v160\n"
    "v_exp_f32 v161, v161\n"
    "v_exp_f32 v162, v162\n"
    "v_exp_f32 v163, v163\n"
    "v_mfma_f32_32x32x16_bf16 v[176:191], v[116:119], v[60:63], v[176:191]\n"
    "v_exp_f32 v164, v164\n"
    "v_exp_f32 v165, v165\n"
    "v_exp_f32 v166, v166\n"
    "v_exp_f32 v167, v167\n"
    "v_smfmac_f32_32x32x32_bf16 v[208:223], v[120:123], v[144:151], v16\n"
    "buffer_load_dwordx4 v[108:111], v5, s[40:43], s52 offen offset:3072 sc1\n"
    "v_exp_f32 v168, v168\n"
    "v_exp_f32 v169, v169\n"
    "v_exp_f32 v170, v170\n"
    "v_exp_f32 v171, v171\n"
    "v_smfmac_f32_32x32x32_bf16 v[208:223], v[124:127], v[152:159], v16\n"
    "s_add_u32 s52, s52, 61440\n"
    "v_exp_f32 v172, v172\n"
    "v_exp_f32 v173, v173\n"
    "v_exp_f32 v174, v174\n"
    "v_exp_f32 v175, v175\n"
    "s_waitcnt vmcnt(11)\n"
    "v_mfma_f32_32x32x16_bf16 v[128:143], v[64:67], v[32:35], 0\n"
    "v_exp_f32 v176, v176\n"
    "v_exp_f32 v177, v177\n"
    "v_exp_f32 v178, v178\n"
    "v_exp_f32 v179, v179\n"
    "s_waitcnt vmcnt(10)\n"
    "v_mfma_f32_32x32x16_bf16 v[128:143], v[68:71], v[36:39], v[128:143]\n"
    "v_exp_f32 v180, v180\n"
    "v_exp_f32 v181, v181\n"
    "v_exp_f32 v182, v182\n"
    "v_exp_f32 v183, v183\n"
    "v_smfmac_f32_32x32x32_bf16 v[224:239], v[120:123], v[160:167], v16\n"
    "buffer_load_dwordx4 v[112:115], v4, s[40:43], s52 offen offset:0 sc1\n"
    "v_exp_f32 v184, v184\n"
    "v_exp_f32 v185, v185\n"
    "v_exp_f32 v186, v186\n"
    "v_exp_f32 v187, v187\n"
    "v_smfmac_f32_32x32x32_bf16 v[224:239], v[124:127], v[168:175], v16\n"
    "v_exp_f32 v188, v188\n"
    "v_exp_f32 v189, v189\n"
    "v_exp_f32 v190, v190\n"
    "v_exp_f32 v191, v191\n"
    "v_mfma_f32_32x32x16_bf16 v[144:159], v[64:67], v[40:43], 0\n"
    "v_exp_f32 v128, v128\n"
    "v_exp_f32 v129, v129\n"
    "v_exp_f32 v130, v130\n"
    "v_exp_f32 v131, v131\n"
    "v_mfma_f32_32x32x16_bf16 v[144:159], v[68:71], v[44:47], v[144:159]\n"
    "v_exp_f32 v132, v132\n"
    "v_exp_f32 v133, v133\n"
    "v_exp_f32 v134, v134\n"
    "v_exp_f32 v135, v135\n"
    "v_smfmac_f32_32x32x32_bf16 v[240:255], v[120:123], v[176:183], v16\n"
    "buffer_load_dwordx4 v[116:119], v4, s[40:43], s52 offen offset:1024 sc1\n"
    "v_exp_f32 v136, v136\n"
    "v_exp_f32 v137, v137\n"
    "v_exp_f32 v138, v138\n"
    "v_exp_f32 v139, v139\n"
    "v_smfmac_f32_32x32x32_bf16 v[240:255], v[124:127], v[184:191], v16\n"
    "v_exp_f32 v140, v140\n"
    "v_exp_f32 v141, v141\n"
    "v_exp_f32 v142, v142\n"
    "v_exp_f32 v143, v143\n"
    "v_mfma_f32_32x32x16_bf16 v[160:175], v[64:67], v[48:51], 0\n"
    "v_exp_f32 v144, v144\n"
    "v_exp_f32 v145, v145\n"
    "v_exp_f32 v146, v146\n"
    "v_exp_f32 v147, v147\n"
    "v_mfma_f32_32x32x16_bf16 v[160:175], v[68:71], v[52:55], v[160:175]\n"
    "s_waitcnt vmcnt(10)\n"
    "v_exp_f32 v148, v148\n"
    "v_exp_f32 v149, v149\n"
    "v_exp_f32 v150, v150\n"
    "v_exp_f32 v151, v151\n"
    "v_smfmac_f32_32x32x32_bf16 v[192:207], v[72:75], v[128:135], v16\n"
    "buffer_load_dwordx4 v[120:123], v5, s[40:43], s52 offen offset:2048 sc1\n"
    "v_exp_f32 v152, v152\n"
    "v_exp_f32 v153, v153\n"
    "v_exp_f32 v154, v154\n"
    "v_exp_f32 v155, v155\n"
    "v_smfmac_f32_32x32x32_bf16 v[192:207], v[76:79], v[136:143], v16\n"
    "v_exp_f32 v156, v156\n"
    "v_exp_f32 v157, v157\n"
    "v_exp_f32 v158, v158\n"
    "v_exp_f32 v159, v159\n"
    "v_mfma_f32_32x32x16_bf16 v[176:191], v[64:67], v[56:59], 0\n"
    "v_exp_f32 v160, v160\n"
    "v_exp_f32 v161, v161\n"
    "v_exp_f32 v162, v162\n"
    "v_exp_f32 v163, v163\n"
    "v_mfma_f32_32x32x16_bf16 v[176:191], v[68:71], v[60:63], v[176:191]\n"
    "v_exp_f32 v164, v164\n"
    "v_exp_f32 v165, v165\n"
    "v_exp_f32 v166, v166\n"
    "v_exp_f32 v167, v167\n"
    "v_smfmac_f32_32x32x32_bf16 v[208:223], v[72:75], v[144:151], v16\n"
    "buffer_load_dwordx4 v[124:127], v5, s[40:43], s52 offen offset:3072 sc1\n"
    "v_exp_f32 v168, v168\n"
    "v_exp_f32 v169, v169\n"
    "v_exp_f32 v170, v170\n"
    "v_exp_f32 v171, v171\n"
    "v_smfmac_f32_32x32x32_bf16 v[208:223], v[76:79], v[152:159], v16\n"
    "s_add_u32 s52, s52, 61440\n"
    "v_exp_f32 v172, v172\n"
    "v_exp_f32 v173, v173\n"
    "v_exp_f32 v174, v174\n"
    "v_exp_f32 v175, v175\n"
    "s_waitcnt vmcnt(11)\n"
    "v_mfma_f32_32x32x16_bf16 v[128:143], v[80:83], v[32:35], 0\n"
    "v_exp_f32 v176, v176\n"
    "v_exp_f32 v177, v177\n"
    "v_exp_f32 v178, v178\n"
    "v_exp_f32 v179, v179\n"
    "s_waitcnt vmcnt(10)\n"
    "v_mfma_f32_32x32x16_bf16 v[128:143], v[84:87], v[36:39], v[128:143]\n"
    "v_exp_f32 v180, v180\n"
    "v_exp_f32 v181, v181\n"
    "v_exp_f32 v182, v182\n"
    "v_exp_f32 v183, v183\n"
    "v_smfmac_f32_32x32x32_bf16 v[224:239], v[72:75], v[160:167], v16\n"
    "buffer_load_dwordx4 v[64:67], v4, s[40:43], s52 offen offset:0 sc1\n"
    "v_exp_f32 v184, v184\n"
    "v_exp_f32 v185, v185\n"
    "v_exp_f32 v186, v186\n"
    "v_exp_f32 v187, v187\n"
    "v_smfmac_f32_32x32x32_bf16 v[224:239], v[76:79], v[168:175], v16\n"
    "v_exp_f32 v188, v188\n"
    "v_exp_f32 v189, v189\n"
    "v_exp_f32 v190, v190\n"
    "v_exp_f32 v191, v191\n"
    "v_mfma_f32_32x32x16_bf16 v[144:159], v[80:83], v[40:43], 0\n"
    "v_exp_f32 v128, v128\n"
    "v_exp_f32 v129, v129\n"
    "v_exp_f32 v130, v130\n"
    "v_exp_f32 v131, v131\n"
    "v_mfma_f32_32x32x16_bf16 v[144:159], v[84:87], v[44:47], v[144:159]\n"
    "v_exp_f32 v132, v132\n"
    "v_exp_f32 v133, v133\n"
    "v_exp_f32 v134, v134\n"
    "v_exp_f32 v135, v135\n"
    "v_smfmac_f32_32x32x32_bf16 v[240:255], v[72:75], v[176:183], v16\n"
    "buffer_load_dwordx4 v[68:71], v4, s[40:43], s52 offen offset:1024 sc1\n"
    "v_exp_f32 v136, v136\n"
    "v_exp_f32 v137, v137\n"
    "v_exp_f32 v138, v138\n"
    "v_exp_f32 v139, v139\n"
    "v_smfmac_f32_32x32x32_bf16 v[240:255], v[76:79], v[184:191], v16\n"
    "v_exp_f32 v140, v140\n"
    "v_exp_f32 v141, v141\n"
    "v_exp_f32 v142, v142\n"
    "v_exp_f32 v143, v143\n"
    "v_mfma_f32_32x32x16_bf16 v[160:175], v[80:83], v[48:51], 0\n"
    "v_exp_f32 v144, v144\n"
    "v_exp_f32 v145, v145\n"
    "v_exp_f32 v146, v146\n"
    "v_exp_f32 v147, v147\n"
    "v_mfma_f32_32x32x16_bf16 v[160:175], v[84:87], v[52:55], v[160:175]\n"
    "s_waitcnt vmcnt(10)\n"
    "v_exp_f32 v148, v148\n"
    "v_exp_f32 v149, v149\n"
    "v_exp_f32 v150, v150\n"
    "v_exp_f32 v151, v151\n"
    "v_smfmac_f32_32x32x32_bf16 v[192:207], v[88:91], v[128:135], v16\n"
    "buffer_load_dwordx4 v[72:75], v5, s[40:43], s52 offen offset:2048 sc1\n"
    "v_exp_f32 v152, v152\n"
    "v_exp_f32 v153, v153\n"
    "v_exp_f32 v154, v154\n"
    "v_exp_f32 v155, v155\n"
    "v_smfmac_f32_32x32x32_bf16 v[192:207], v[92:95], v[136:143], v16\n"
    "v_exp_f32 v156, v156\n"
    "v_exp_f32 v157, v157\n"
    "v_exp_f32 v158, v158\n"
    "v_exp_f32 v159, v159\n"
    "v_mfma_f32_32x32x16_bf16 v[176:191], v[80:83], v[56:59], 0\n"
    "v_exp_f32 v160, v160\n"
    "v_exp_f32 v161, v161\n"
    "v_exp_f32 v162, v162\n"
    "v_exp_f32 v163, v163\n"
    "v_mfma_f32_32x32x16_bf16 v[176:191], v[84:87], v[60:63], v[176:191]\n"
    "v_exp_f32 v164, v164\n"
    "v_exp_f32 v165, v165\n"
    "v_exp_f32 v166, v166\n"
    "v_exp_f32 v167, v167\n"
    "v_smfmac_f32_32x32x32_bf16 v[208:223], v[88:91], v[144:151], v16\n"
    "buffer_load_dwordx4 v[76:79], v5, s[40:43], s52 offen offset:3072 sc1\n"
    "v_exp_f32 v168, v168\n"
    "v_exp_f32 v169, v169\n"
    "v_exp_f32 v170, v170\n"
    "v_exp_f32 v171, v171\n"
    "v_smfmac_f32_32x32x32_bf16 v[208:223], v[92:95], v[152:159], v16\n"
    "s_add_u32 s52, s52, 61440\n"
    "v_exp_f32 v172, v172\n"
    "v_exp_f32 v173, v173\n"
    "v_exp_f32 v174, v174\n"
    "v_exp_f32 v175, v175\n"
    "s_waitcnt vmcnt(11)\n"
    "v_mfma_f32_32x32x16_bf16 v[128:143], v[96:99], v[32:35], 0\n"
    "v_exp_f32 v176, v176\n"
    "v_exp_f32 v177, v177\n"
    "v_exp_f32 v178, v178\n"
    "v_exp_f32 v179, v179\n"
    "s_waitcnt vmcnt(10)\n"
    "v_mfma_f32_32x32x16_bf16 v[128:143], v[100:103], v[36:39], v[128:143]\n"
    "v_exp_f32 v180, v180\n"
    "v_exp_f32 v181, v181\n"
    "v_exp_f32 v182, v182\n"
    "v_exp_f32 v183, v183\n"
    "v_smfmac_f32_32x32x32_bf16 v[224:239], v[88:91], v[160:167], v16\n"
    "buffer_load_dwordx4 v[80:83], v4, s[40:43], s52 offen offset:0 sc1\n"
    "v_exp_f32 v184, v184\n"
    "v_exp_f32 v185, v185\n"
    "v_exp_f32 v186, v186\n"
    "v_exp_f32 v187, v187\n"
    "v_smfmac_f32_32x32x32_bf16 v[224:239], v[92:95], v[168:175], v16\n"
    "v_exp_f32 v188, v188\n"
    "v_exp_f32 v189, v189\n"
    "v_exp_f32 v190, v190\n"
    "v_exp_f32 v191, v191\n"
    "v_mfma_f32_32x32x16_bf16 v[144:159], v[96:99], v[40:43], 0\n"
    "v_exp_f32 v128, v128\n"
    "v_exp_f32 v129, v129\n"
    "v_exp_f32 v130, v130\n"
    "v_exp_f32 v131, v131\n"
    "v_mfma_f32_32x32x16_bf16 v[144:159], v[100:103], v[44:47], v[144:159]\n"
    "v_exp_f32 v132, v132\n"
    "v_exp_f32 v133, v133\n"
    "v_exp_f32 v134, v134\n"
    "v_exp_f32 v135, v135\n"
    "v_smfmac_f32_32x32x32_bf16 v[240:255], v[88:91], v[176:183], v16\n"
    "buffer_load_dwordx4 v[84:87], v4, s[40:43], s52 offen offset:1024 sc1\n"
    "v_exp_f32 v136, v136\n"
    "v_exp_f32 v137, v137\n"
    "v_exp_f32 v138, v138\n"
    "v_exp_f32 v139, v139\n"
    "v_smfmac_f32_32x32x32_bf16 v[240:255], v[92:95], v[184:191], v16\n"
    "v_exp_f32 v140, v140\n"
    "v_exp_f32 v141, v141\n"
    "v_exp_f32 v142, v142\n"
    "v_exp_f32 v143, v143\n"
    "v_mfma_f32_32x32x16_bf16 v[160:175], v[96:99], v[48:51], 0\n"
    "v_exp_f32 v144, v144\n"
    "v_exp_f32 v145, v145\n"
    "v_exp_f32 v146, v146\n"
    "v_exp_f32 v147, v147\n"
    "v_mfma_f32_32x32x16_bf16 v[160:175], v[100:103], v[52:55], v[160:175]\n"
    "s_waitcnt vmcnt(10)\n"
    "v_exp_f32 v148, v148\n"
    "v_exp_f32 v149, v149\n"
    "v_exp_f32 v150, v150\n"
    "v_exp_f32 v151, v151\n"
    "v_smfmac_f32_32x32x32_bf16 v[192:207], v[104:107], v[128:135], v16\n"
    "buffer_load_dwordx4 v[88:91], v5, s[40:43], s52 offen offset:2048 sc1\n"
    "v_exp_f32 v152, v152\n"
    "v_exp_f32 v153, v153\n"
    "v_exp_f32 v154, v154\n"
    "v_exp_f32 v155, v155\n"
    "v_smfmac_f32_32x32x32_bf16 v[192:207], v[108:111], v[136:143], v16\n"
    "v_exp_f32 v156, v156\n"
    "v_exp_f32 v157, v157\n"
    "v_exp_f32 v158, v158\n"
    "v_exp_f32 v159, v159\n"
    "v_mfma_f32_32x32x16_bf16 v[176:191], v[96:99], v[56:59], 0\n"
    "v_exp_f32 v160, v160\n"
    "v_exp_f32 v161, v161\n"
    "v_exp_f32 v162, v162\n"
    "v_exp_f32 v163, v163\n"
    "v_mfma_f32_32x32x16_bf16 v[176:191], v[100:103], v[60:63], v[176:191]\n"
    "v_exp_f32 v164, v164\n"
    "v_exp_f32 v165, v165\n"
    "v_exp_f32 v166, v166\n"
    "v_exp_f32 v167, v167\n"
    "v_smfmac_f32_32x32x32_bf16 v[208:223], v[104:107], v[144:151], v16\n"
    "buffer_load_dwordx4 v[92:95], v5, s[40:43], s52 offen offset:3072 sc1\n"
    "v_exp_f32 v168, v168\n"
    "v_exp_f32 v169, v169\n"
    "v_exp_f32 v170, v170\n"
    "v_exp_f32 v171, v171\n"
    "v_smfmac_f32_32x32x32_bf16 v[208:223], v[108:111], v[152:159], v16\n"
    "s_add_u32 s52, s52, 61440\n"
    "v_exp_f32 v172, v172\n"
    "v_exp_f32 v173, v173\n"
    "v_exp_f32 v174, v174\n"
    "v_exp_f32 v175, v175\n"
    "s_waitcnt vmcnt(11)\n"
    "v_mfma_f32_32x32x16_bf16 v[128:143], v[112:115], v[32:35], 0\n"
    "v_exp_f32 v176, v176\n"
    "v_exp_f32 v177, v177\n"
    "v_exp_f32 v178, v178\n"
    "v_exp_f32 v179, v179\n"
    "s_waitcnt vmcnt(10)\n"
    "v_mfma_f32_32x32x16_bf16 v[128:143], v[116:119], v[36:39], v[128:143]\n"
    "v_exp_f32 v180, v180\n"
    "v_exp_f32 v181, v181\n"
    "v_exp_f32 v182, v182\n"
    "v_exp_f32 v183, v183\n"
    "v_smfmac_f32_32x32x32_bf16 v[224:239], v[104:107], v[160:167], v16\n"
    "buffer_load_dwordx4 v[96:99], v4, s[40:43], s52 offen offset:0 sc1\n"
    "v_exp_f32 v184, v184\n"
    "v_exp_f32 v185, v185\n"
    "v_exp_f32 v186, v186\n"
    "v_exp_f32 v187, v187\n"
    "v_smfmac_f32_32x32x32_bf16 v[224:239], v[108:111], v[168:175], v16\n"
    "v_exp_f32 v188, v188\n"
    "v_exp_f32 v189, v189\n"
    "v_exp_f32 v190, v190\n"
    "v_exp_f32 v191, v191\n"
    "v_mfma_f32_32x32x16_bf16 v[144:159], v[112:115], v[40:43], 0\n"
    "v_exp_f32 v128, v128\n"
    "v_exp_f32 v129, v129\n"
    "v_exp_f32 v130, v130\n"
    "v_exp_f32 v131, v131\n"
    "v_mfma_f32_32x32x16_bf16 v[144:159], v[116:119], v[44:47], v[144:159]\n"
    "v_exp_f32 v132, v132\n"
    "v_exp_f32 v133, v133\n"
    "v_exp_f32 v134, v134\n"
    "v_exp_f32 v135, v135\n"
    "v_smfmac_f32_32x32x32_bf16 v[240:255], v[104:107], v[176:183], v16\n"
    "buffer_load_dwordx4 v[100:103], v4, s[40:43], s52 offen offset:1024 sc1\n"
    "v_exp_f32 v136, v136\n"
    "v_exp_f32 v137, v137\n"
    "v_exp_f32 v138, v138\n"
    "v_exp_f32 v139, v139\n"
    "v_smfmac_f32_32x32x32_bf16 v[240:255], v[108:111], v[184:191], v16\n"
    "v_exp_f32 v140, v140\n"
    "v_exp_f32 v141, v141\n"
    "v_exp_f32 v142, v142\n"
    "v_exp_f32 v143, v143\n"
    "v_mfma_f32_32x32x16_bf16 v[160:175], v[112:115], v[48:51], 0\n"
    "v_exp_f32 v144, v144\n"
    "v_exp_f32 v145, v145\n"
    "v_exp_f32 v146, v146\n"
    "v_exp_f32 v147, v147\n"
    "v_mfma_f32_32x32x16_bf16 v[160:175], v[116:119], v[52:55], v[160:175]\n"
    "s_waitcnt vmcnt(10)\n"
    "v_exp_f32 v148, v148\n"
    "v_exp_f32 v149, v149\n"
    "v_exp_f32 v150, v150\n"
    "v_exp_f32 v151, v151\n"
    "v_smfmac_f32_32x32x32_bf16 v[192:207], v[120:123], v[128:135], v16\n"
    "buffer_load_dwordx4 v[104:107], v5, s[40:43], s52 offen offset:2048 sc1\n"
    "v_exp_f32 v152, v152\n"
    "v_exp_f32 v153, v153\n"
    "v_exp_f32 v154, v154\n"
    "v_exp_f32 v155, v155\n"
    "v_smfmac_f32_32x32x32_bf16 v[192:207], v[124:127], v[136:143], v16\n"
    "v_exp_f32 v156, v156\n"
    "v_exp_f32 v157, v157\n"
    "v_exp_f32 v158, v158\n"
    "v_exp_f32 v159, v159\n"
    "v_mfma_f32_32x32x16_bf16 v[176:191], v[112:115], v[56:59], 0\n"
    "v_exp_f32 v160, v160\n"
    "v_exp_f32 v161, v161\n"
    "v_exp_f32 v162, v162\n"
    "v_exp_f32 v163, v163\n"
    "v_mfma_f32_32x32x16_bf16 v[176:191], v[116:119], v[60:63], v[176:191]\n"
    "v_exp_f32 v164, v164\n"
    "v_exp_f32 v165, v165\n"
    "v_exp_f32 v166, v166\n"
    "v_exp_f32 v167, v167\n"
    "v_smfmac_f32_32x32x32_bf16 v[208:223], v[120:123], v[144:151], v16\n"
    "buffer_load_dwordx4 v[108:111], v5, s[40:43], s52 offen offset:3072 sc1\n"
    "v_exp_f32 v168, v168\n"
    "v_exp_f32 v169, v169\n"
    "v_exp_f32 v170, v170\n"
    "v_exp_f32 v171, v171\n"
    "v_smfmac_f32_32x32x32_bf16 v[208:223], v[124:127], v[152:159], v16\n"
    "s_add_u32 s52, s52, 61440\n"
    "v_exp_f32 v172, v172\n"
    "v_exp_f32 v173, v173\n"
    "v_exp_f32 v174, v174\n"
    "v_exp_f32 v175, v175\n"
    "s_waitcnt vmcnt(11)\n"
    "v_mfma_f32_32x32x16_bf16 v[128:143], v[64:67], v[32:35], 0\n"
    "v_exp_f32 v176, v176\n"
    "v_exp_f32 v177, v177\n"
    "v_exp_f32 v178, v178\n"
    "v_exp_f32 v179, v179\n"
    "s_waitcnt vmcnt(10)\n"
    "v_mfma_f32_32x32x16_bf16 v[128:143], v[68:71], v[36:39], v[128:143]\n"
    "v_exp_f32 v180, v180\n"
    "v_exp_f32 v181, v181\n"
    "v_exp_f32 v182, v182\n"
    "v_exp_f32 v183, v183\n"
    "v_smfmac_f32_32x32x32_bf16 v[224:239], v[120:123], v[160:167], v16\n"
    "buffer_load_dwordx4 v[112:115], v4, s[40:43], s52 offen offset:0 sc1\n"
    "v_exp_f32 v184, v184\n"
    "v_exp_f32 v185, v185\n"
    "v_exp_f32 v186, v186\n"
    "v_exp_f32 v187, v187\n"
    "v_smfmac_f32_32x32x32_bf16 v[224:239], v[124:127], v[168:175], v16\n"
    "v_exp_f32 v188, v188\n"
    "v_exp_f32 v189, v189\n"
    "v_exp_f32 v190, v190\n"
    "v_exp_f32 v191, v191\n"
    "v_mfma_f32_32x32x16_bf16 v[144:159], v[64:67], v[40:43], 0\n"
    "v_exp_f32 v128, v128\n"
    "v_exp_f32 v129, v129\n"
    "v_exp_f32 v130, v130\n"
    "v_exp_f32 v131, v131\n"
    "v_mfma_f32_32x32x16_bf16 v[144:159], v[68:71], v[44:47], v[144:159]\n"
    "v_exp_f32 v132, v132\n"
    "v_exp_f32 v133, v133\n"
    "v_exp_f32 v134, v134\n"
    "v_exp_f32 v135, v135\n"
    "v_smfmac_f32_32x32x32_bf16 v[240:255], v[120:123], v[176:183], v16\n"
    "buffer_load_dwordx4 v[116:119], v4, s[40:43], s52 offen offset:1024 sc1\n"
    "v_exp_f32 v136, v136\n"
    "v_exp_f32 v137, v137\n"
    "v_exp_f32 v138, v138\n"
    "v_exp_f32 v139, v139\n"
    "v_smfmac_f32_32x32x32_bf16 v[240:255], v[124:127], v[184:191], v16\n"
    "v_exp_f32 v140, v140\n"
    "v_exp_f32 v141, v141\n"
    "v_exp_f32 v142, v142\n"
    "v_exp_f32 v143, v143\n"
    "v_mfma_f32_32x32x16_bf16 v[160:175], v[64:67], v[48:51], 0\n"
    "v_exp_f32 v144, v144\n"
    "v_exp_f32 v145, v145\n"
    "v_exp_f32 v146, v146\n"
    "v_exp_f32 v147, v147\n"
    "v_mfma_f32_32x32x16_bf16 v[160:175], v[68:71], v[52:55], v[160:175]\n"
    "s_waitcnt vmcnt(10)\n"
    "v_exp_f32 v148, v148\n"
    "v_exp_f32 v149, v149\n"
    "v_exp_f32 v150, v150\n"
    "v_exp_f32 v151, v151\n"
    "v_smfmac_f32_32x32x32_bf16 v[192:207], v[72:75], v[128:135], v16\n"
    "buffer_load_dwordx4 v[120:123], v5, s[40:43], s52 offen offset:2048 sc1\n"
    "v_exp_f32 v152, v152\n"
    "v_exp_f32 v153, v153\n"
    "v_exp_f32 v154, v154\n"
    "v_exp_f32 v155, v155\n"
    "v_smfmac_f32_32x32x32_bf16 v[192:207], v[76:79], v[136:143], v16\n"
    "v_exp_f32 v156, v156\n"
    "v_exp_f32 v157, v157\n"
    "v_exp_f32 v158, v158\n"
    "v_exp_f32 v159, v159\n"
    "v_mfma_f32_32x32x16_bf16 v[176:191], v[64:67], v[56:59], 0\n"
    "v_exp_f32 v160, v160\n"
    "v_exp_f32 v161, v161\n"
    "v_exp_f32 v162, v162\n"
    "v_exp_f32 v163, v163\n"
    "v_mfma_f32_32x32x16_bf16 v[176:191], v[68:71], v[60:63], v[176:191]\n"
    "v_exp_f32 v164, v164\n"
    "v_exp_f32 v165, v165\n"
    "v_exp_f32 v166, v166\n"
    "v_exp_f32 v167, v167\n"
    "v_smfmac_f32_32x32x32_bf16 v[208:223], v[72:75], v[144:151], v16\n"
    "buffer_load_dwordx4 v[124:127], v5, s[40:43], s52 offen offset:3072 sc1\n"
    "v_exp_f32 v168, v168\n"
    "v_exp_f32 v169, v169\n"
    "v_exp_f32 v170, v170\n"
    "v_exp_f32 v171, v171\n"
    "v_smfmac_f32_32x32x32_bf16 v[208:223], v[76:79], v[152:159], v16\n"
    "s_waitcnt vmcnt(10)\n"
    "v_exp_f32 v172, v172\n"
    "v_exp_f32 v173, v173\n"
    "v_exp_f32 v174, v174\n"
    "v_exp_f32 v175, v175\n"
    "v_mfma_f32_32x32x16_bf16 v[128:143], v[80:83], v[32:35], 0\n"
    "v_exp_f32 v176, v176\n"
    "v_exp_f32 v177, v177\n"
    "v_exp_f32 v178, v178\n"
    "v_exp_f32 v179, v179\n"
    "v_mfma_f32_32x32x16_bf16 v[128:143], v[84:87], v[36:39], v[128:143]\n"
    "v_exp_f32 v180, v180\n"
    "v_exp_f32 v181, v181\n"
    "v_exp_f32 v182, v182\n"
    "v_exp_f32 v183, v183\n"
    "v_smfmac_f32_32x32x32_bf16 v[224:239], v[72:75], v[160:167], v16\n"
    "v_exp_f32 v184, v184\n"
    "v_exp_f32 v185, v185\n"
    "v_exp_f32 v186, v186\n"
    "v_exp_f32 v187, v187\n"
    "v_smfmac_f32_32x32x32_bf16 v[224:239], v[76:79], v[168:175], v16\n"
    "v_exp_f32 v188, v188\n"
    "v_exp_f32 v189, v189\n"
    "v_exp_f32 v190, v190\n"
    "v_exp_f32 v191, v191\n"
    "v_mfma_f32_32x32x16_bf16 v[144:159], v[80:83], v[40:43], 0\n"
    "v_exp_f32 v128, v128\n"
    "v_exp_f32 v129, v129\n"
    "v_exp_f32 v130, v130\n"
    "v_exp_f32 v131, v131\n"
    "v_mfma_f32_32x32x16_bf16 v[144:159], v[84:87], v[44:47], v[144:159]\n"
    "v_exp_f32 v132, v132\n"
    "v_exp_f32 v133, v133\n"
    "v_exp_f32 v134, v134\n"
    "v_exp_f32 v135, v135\n"
    "v_smfmac_f32_32x32x32_bf16 v[240:255], v[72:75], v[176:183], v16\n"
    "v_exp_f32 v136, v136\n"
    "v_exp_f32 v137, v137\n"
    "v_exp_f32 v138, v138\n"
    "v_exp_f32 v139, v139\n"
    "v_smfmac_f32_32x32x32_bf16 v[240:255], v[76:79], v[184:191], v16\n"
    "v_exp_f32 v140, v140\n"
    "v_exp_f32 v141, v141\n"
    "v_exp_f32 v142, v142\n"
    "v_exp_f32 v143, v143\n"
    "v_mfma_f32_32x32x16_bf16 v[160:175], v[80:83], v[48:51], 0\n"
    "v_exp_f32 v144, v144\n"
    "v_exp_f32 v145, v145\n"
    "v_exp_f32 v146, v146\n"
    "v_exp_f32 v147, v147\n"
    "v_mfma_f32_32x32x16_bf16 v[160:175], v[84:87], v[52:55], v[160:175]\n"
    "s_waitcnt vmcnt(8)\n"
    "v_exp_f32 v148, v148\n"
    "v_exp_f32 v149, v149\n"
    "v_exp_f32 v150, v150\n"
    "v_exp_f32 v151, v151\n"
    "v_smfmac_f32_32x32x32_bf16 v[192:207], v[88:91], v[128:135], v16\n"
    "v_exp_f32 v152, v152\n"
    "v_exp_f32 v153, v153\n"
    "v_exp_f32 v154, v154\n"
    "v_exp_f32 v155, v155\n"
    "v_smfmac_f32_32x32x32_bf16 v[192:207], v[92:95], v[136:143], v16\n"
    "v_exp_f32 v156, v156\n"
    "v_exp_f32 v157, v157\n"
    "v_exp_f32 v158, v158\n"
    "v_exp_f32 v159, v159\n"
    "v_mfma_f32_32x32x16_bf16 v[176:191], v[80:83], v[56:59], 0\n"
    "ds_read_b128 v[18:21], v26 offset:0\n"
    "v_exp_f32 v160, v160\n"
    "v_exp_f32 v161, v161\n"
    "v_exp_f32 v162, v162\n"
    "v_exp_f32 v163, v163\n"
    "v_mfma_f32_32x32x16_bf16 v[176:191], v[84:87], v[60:63], v[176:191]\n"
    "v_exp_f32 v164, v164\n"
    "v_exp_f32 v165, v165\n"
    "v_exp_f32 v166, v166\n"
    "v_exp_f32 v167, v167\n"
    "v_smfmac_f32_32x32x32_bf16 v[208:223], v[88:91], v[144:151], v16\n"
    "v_exp_f32 v168, v168\n"
    "v_exp_f32 v169, v169\n"
    "v_exp_f32 v170, v170\n"
    "v_exp_f32 v171, v171\n"
    "v_smfmac_f32_32x32x32_bf16 v[208:223], v[92:95], v[152:159], v16\n"
    "s_waitcnt vmcnt(6)\n"
    "v_exp_f32 v172, v172\n"
    "v_exp_f32 v173, v173\n"
    "v_exp_f32 v174, v174\n"
    "v_exp_f32 v175, v175\n"
    "v_mfma_f32_32x32x16_bf16 v[128:143], v[96:99], v[32:35], 0\n"
    "ds_read_b128 v[22:25], v26 offset:16\n"
    "v_exp_f32 v176, v176\n"
    "v_exp_f32 v177, v177\n"
    "v_exp_f32 v178, v178\n"
    "v_exp_f32 v179, v179\n"
    "v_mfma_f32_32x32x16_bf16 v[128:143], v[100:103], v[36:39], v[128:143]\n"
    "v_exp_f32 v180, v180\n"
    "v_exp_f32 v181, v181\n"
    "v_exp_f32 v182, v182\n"
    "v_exp_f32 v183, v183\n"
    "v_smfmac_f32_32x32x32_bf16 v[224:239], v[88:91], v[160:167], v16\n"
    "v_exp_f32 v184, v184\n"
    "v_exp_f32 v185, v185\n"
    "v_exp_f32 v186, v186\n"
    "v_exp_f32 v187, v187\n"
    "v_smfmac_f32_32x32x32_bf16 v[224:239], v[92:95], v[168:175], v16\n"
    "s_waitcnt vmcnt(2)\n"
    "v_exp_f32 v188, v188\n"
    "v_exp_f32 v189, v189\n"
    "v_exp_f32 v190, v190\n"
    "v_exp_f32 v191, v191\n"
    "v_mfma_f32_32x32x16_bf16 v[144:159], v[112:115], v[32:35], 0\n"
    "s_mov_b32 s63, 0x100000\n"
    ".Lw2_8:\n"
    "s_waitcnt lgkmcnt(0)\n"
    "v_min3_u32 v18, v18, v19, v20\n"
    "v_min3_u32 v22, v22, v23, v24\n"
    "v_min_u32 v22, v22, v25\n"
    "v_add_u32 v22, 4, v22\n"
    "v_min3_i32 v18, v18, v21, v22\n"
    "v_cmp_gt_i32 vcc, 7, v18\n"
    "s_cbranch_vccz .Lw2d_10\n"
    "ds_read_b128 v[18:21], v26 offset:0\n"
    "ds_read_b128 v[22:25], v26 offset:16\n"
    "s_sub_u32 s63, s63, 1\n"
    "s_cbranch_scc0 .Lw2_8\n"
    "s_waitcnt lgkmcnt(0)\n"
    ".Lw2d_10:\n"
    "v_exp_f32 v128, v128\n"
    "v_exp_f32 v129, v129\n"
    "v_exp_f32 v130, v130\n"
    "v_exp_f32 v131, v131\n"
    "v_mfma_f32_32x32x16_bf16 v[144:159], v[116:119], v[36:39], v[144:159]\n"
    "v_exp_f32 v132, v132\n"
    "v_exp_f32 v133, v133\n"
    "v_exp_f32 v134, v134\n"
    "v_exp_f32 v135, v135\n"
    "v_smfmac_f32_32x32x32_bf16 v[240:255], v[88:91], v[176:183], v16\n"
    "v_exp_f32 v136, v136\n"
    "v_exp_f32 v137, v137\n"
    "v_exp_f32 v138, v138\n"
    "v_exp_f32 v139, v139\n"
    "v_smfmac_f32_32x32x32_bf16 v[240:255], v[92:95], v[184:191], v16\n"
    "v_exp_f32 v140, v140\n"
    "v_exp_f32 v141, v141\n"
    "v_exp_f32 v142, v142\n"
    "v_exp_f32 v143, v143\n"
    "v_mfma_f32_32x32x16_bf16 v[160:175], v[96:99], v[40:43], 0\n"
    "v_exp_f32 v144, v144\n"
    "v_exp_f32 v145, v145\n"
    "v_exp_f32 v146, v146\n"
    "v_exp_f32 v147, v147\n"
    "v_mfma_f32_32x32x16_bf16 v[160:175], v[100:103], v[44:47], v[160:175]\n"
    "v_exp_f32 v148, v148\n"
    "v_exp_f32 v149, v149\n"
    "v_exp_f32 v150, v150\n"
    "v_exp_f32 v151, v151\n"
    "v_smfmac_f32_32x32x32_bf16 v[192:207], v[104:107], v[128:135], v16\n"
    "v_exp_f32 v152, v152\n"
    "v_exp_f32 v153, v153\n"
    "v_exp_f32 v154, v154\n"
    "v_exp_f32 v155, v155\n"
    "v_smfmac_f32_32x32x32_bf16 v[192:207], v[108:111], v[136:143], v16\n"
    "v_exp_f32 v156, v156\n"
    "v_exp_f32 v157, v157\n"
    "v_exp_f32 v158, v158\n"
    "v_exp_f32 v159, v159\n"
    "v_mfma_f32_32x32x16_bf16 v[176:191], v[112:115], v[40:43], 0\n"
    "v_exp_f32 v160, v160\n"
    "v_exp_f32 v161, v161\n"
    "v_exp_f32 v162, v162\n"
    "v_exp_f32 v163, v163\n"
    "v_mfma_f32_32x32x16_bf16 v[176:191], v[116:119], v[44:47], v[176:191]\n"
    "s_waitcnt vmcnt(0)\n"
    "v_exp_f32 v164, v164\n"
    "v_exp_f32 v165, v165\n"
    "v_exp_f32 v166, v166\n"
    "v_exp_f32 v167, v167\n"
    "v_smfmac_f32_32x32x32_bf16 v[192:207], v[120:123], v[144:151], v16\n"
    "v_exp_f32 v168, v168\n"
    "v_exp_f32 v169, v169\n"
    "v_exp_f32 v170, v170\n"
    "v_exp_f32 v171, v171\n"
    "v_smfmac_f32_32x32x32_bf16 v[192:207], v[124:127], v[152:159], v16\n"
    "v_exp_f32 v172, v172\n"
    "v_exp_f32 v173, v173\n"
    "v_exp_f32 v174, v174\n"
    "v_exp_f32 v175, v175\n"
    "v_mfma_f32_32x32x16_bf16 v[128:143], v[96:99], v[48:51], 0\n"
    "v_exp_f32 v176, v176\n"
    "v_exp_f32 v177, v177\n"
    "v_exp_f32 v178, v178\n"
    "v_exp_f32 v179, v179\n"
    "v_mfma_f32_32x32x16_bf16 v[128:143], v[100:103], v[52:55], v[128:143]\n"
    "v_exp_f32 v180, v180\n"
    "v_exp_f32 v181, v181\n"
    "v_exp_f32 v182, v182\n"
    "v_exp_f32 v183, v183\n"
    "v_smfmac_f32_32x32x32_bf16 v[208:223], v[104:107], v[160:167], v16\n"
    "ds_write_b128 v8, v[192:195] offset:0\n"
    "v_exp_f32 v184, v184\n"
    "v_exp_f32 v185, v185\n"
    "v_exp_f32 v186, v186\n"
    "v_exp_f32 v187, v187\n"
    "v_smfmac_f32_32x32x32_bf16 v[208:223], v[108:111], v[168:175], v16\n"
    "ds_write_b128 v8, v[196:199] offset:1024\n"
    "v_exp_f32 v188, v188\n"
    "v_exp_f32 v189, v189\n"
    "v_exp_f32 v190, v190\n"
    "v_exp_f32 v191, v191\n"
    "v_mfma_f32_32x32x16_bf16 v[144:159], v[112:115], v[48:51], 0\n"
    "v_exp_f32 v128, v128\n"
    "v_exp_f32 v129, v129\n"
    "v_exp_f32 v130, v130\n"
    "v_exp_f32 v131, v131\n"
    "v_mfma_f32_32x32x16_bf16 v[144:159], v[116:119], v[52:55], v[144:159]\n"
    "v_exp_f32 v132, v132\n"
    "v_exp_f32 v133, v133\n"
    "v_exp_f32 v134, v134\n"
    "v_exp_f32 v135, v135\n"
    "v_smfmac_f32_32x32x32_bf16 v[208:223], v[120:123], v[176:183], v16\n"
    "ds_write_b128 v8, v[200:203] offset:2048\n"
    "v_exp_f32 v136, v136\n"
    "v_exp_f32 v137, v137\n"
    "v_exp_f32 v138, v138\n"
    "v_exp_f32 v139, v139\n"
    "v_smfmac_f32_32x32x32_bf16 v[208:223], v[124:127], v[184:191], v16\n"
    "ds_write_b128 v8, v[204:207] offset:3072\n"
    "v_exp_f32 v140, v140\n"
    "v_exp_f32 v141, v141\n"
    "v_exp_f32 v142, v142\n"
    "v_exp_f32 v143, v143\n"
    "v_mfma_f32_32x32x16_bf16 v[160:175], v[96:99], v[56:59], 0\n"
    "v_exp_f32 v144, v144\n"
    "v_exp_f32 v145, v145\n"
    "v_exp_f32 v146, v146\n"
    "v_exp_f32 v147, v147\n"
    "v_mfma_f32_32x32x16_bf16 v[160:175], v[100:103], v[60:63], v[160:175]\n"
    "v_exp_f32 v148, v148\n"
    "v_exp_f32 v149, v149\n"
    "v_exp_f32 v150, v150\n"
    "v_exp_f32 v151, v151\n"
    "v_smfmac_f32_32x32x32_bf16 v[224:239], v[104:107], v[128:135], v16\n"
    "ds_write_b128 v8, v[208:211] offset:4096\n"
    "v_exp_f32 v152, v152\n"
    "v_exp_f32 v153, v153\n"
    "v_exp_f32 v154, v154\n"
    "v_exp_f32 v155, v155\n"
    "v_smfmac_f32_32x32x32_bf16 v[224:239], v[108:111], v[136:143], v16\n"
    "ds_write_b128 v8, v[212:215] offset:5120\n"
    "v_exp_f32 v156, v156\n"
    "v_exp_f32 v157, v157\n"
    "v_exp_f32 v158, v158\n"
    "v_exp_f32 v159, v159\n"
    "v_mfma_f32_32x32x16_bf16 v[176:191], v[112:115], v[56:59], 0\n"
    "s_setprio 3\n"
    "v_exp_f32 v160, v160\n"
    "v_exp_f32 v161, v161\n"
    "v_exp_f32 v162, v162\n"
    "v_exp_f32 v163, v163\n"
    "v_mfma_f32_32x32x16_bf16 v[176:191], v[116:119], v[60:63], v[176:191]\n"
    "ds_write_b32 v0, v17 offset:32\n"
    "v_exp_f32 v164, v164\n"
    "v_exp_f32 v165, v165\n"
    "v_exp_f32 v166, v166\n"
    "v_exp_f32 v167, v167\n"
    "v_smfmac_f32_32x32x32_bf16 v[224:239], v[120:123], v[144:151], v16\n"
    "ds_write_b128 v8, v[216:219] offset:6144\n"
    "v_exp_f32 v168, v168\n"
    "v_exp_f32 v169, v169\n"
    "v_exp_f32 v170, v170\n"
    "v_exp_f32 v171, v171\n"
    "v_smfmac_f32_32x32x32_bf16 v[224:239], v[124:127], v[152:159], v16\n"
    "ds_write_b128 v8, v[220:223] offset:7168\n"
    "v_exp_f32 v172, v172\n"
    "v_exp_f32 v173, v173\n"
    "v_exp_f32 v174, v174\n"
    "v_exp_f32 v175, v175\n"
    "v_exp_f32 v176, v176\n"
    "v_exp_f32 v177, v177\n"
    "v_exp_f32 v178, v178\n"
    "v_exp_f32 v179, v179\n"
    "s_nop 2\n"
    "ds_write_b128 v8, v[224:227] offset:8192\n"
    "v_exp_f32 v180, v180\n"
    "v_exp_f32 v181, v181\n"
    "v_exp_f32 v182, v182\n"
    "v_exp_f32 v183, v183\n"
    "v_smfmac_f32_32x32x32_bf16 v[240:255], v[104:107], v[160:167], v16\n"
    "ds_write_b128 v8, v[228:231] offset:9216\n"
    "v_exp_f32 v184, v184\n"
    "v_exp_f32 v185, v185\n"
    "v_exp_f32 v186, v186\n"
    "v_exp_f32 v187, v187\n"
    "v_smfmac_f32_32x32x32_bf16 v[240:255], v[108:111], v[168:175], v16\n"
    "ds_write_b128 v8, v[232:235] offset:10240\n"
    "v_exp_f32 v188, v188\n"
    "v_exp_f32 v189, v189\n"
    "v_exp_f32 v190, v190\n"
    "v_exp_f32 v191, v191\n"
    "ds_write_b128 v8, v[236:239] offset:11264\n"
    "v_smfmac_f32_32x32x32_bf16 v[240:255], v[120:123], v[176:183], v16\n"
    "v_smfmac_f32_32x32x32_bf16 v[240:255], v[124:127], v[184:191], v16\n"
    ".Lcomb:\n"
    "s_cmp_lg_u32 s3, 0\n"
    "s_cselect_b32 s54, 10, 6\n"
    "v_mov_b32 v18, s54\n"
    "ds_write_b32 v0, v18\n"
    "v_mov_b32 v22, 163344\n"
    "s_mov_b32 s63, 0x100000\n"
    ".Lwp_11:\n"
    "ds_read_b128 v[18:21], v22\n"
    "s_waitcnt lgkmcnt(0)\n"
    "v_min3_u32 v23, v18, v19, v20\n"
    "v_min_u32 v23, v23, v21\n"
    "v_cmp_gt_u32 vcc, s54, v23\n"
    "s_cbranch_vccz .Lwpd_12\n"
    "s_sleep 1\n"
    "s_sub_u32 s63, s63, 1\n"
    "s_cbranch_scc0 .Lwp_11\n"
    ".Lwpd_12:\n"
    "ds_read_b128 v[204:207], v9 offset:3072\n"
    "ds_read_b128 v[220:223], v10 offset:3072\n"
    "ds_read_b128 v[236:239], v11 offset:3072\n"
    "ds_read_b128 v[192:195], v9 offset:0\n"
    "ds_read_b128 v[208:211], v10 offset:0\n"
    "ds_read_b128 v[224:227], v11 offset:0\n"
    "ds_read_b128 v[196:199], v9 offset:1024\n"
    "ds_read_b128 v[212:215], v10 offset:1024\n"
    "ds_read_b128 v[228:231], v11 offset:1024\n"
    "ds_read_b128 v[200:203], v9 offset:2048\n"
    "ds_read_b128 v[216:219], v10 offset:2048\n"
    "ds_read_b128 v[232:235], v11 offset:2048\n"
    "v_mov_b32 v150, 0\n"
    "v_mov_b32 v151, 0\n"
    "s_waitcnt lgkmcnt(11)\n"
    "v_add_f32 v252, v252, v204\n"
    "v_add_f32 v253, v253, v205\n"
    "v_add_f32 v254, v254, v206\n"
    "v_add_f32 v255, v255, v207\n"
    "s_waitcnt lgkmcnt(10)\n"
    "v_add_f32 v252, v252, v220\n"
    "v_add_f32 v253, v253, v221\n"
    "v_add_f32 v254, v254, v222\n"
    "v_add_f32 v255, v255, v223\n"
    "s_waitcnt lgkmcnt(9)\n"
    "v_add_f32 v252, v252, v236\n"
    "v_add_f32 v253, v253, v237\n"
    "v_add_f32 v254, v254, v238\n"
    "v_add_f32 v255, v255, v239\n"
    "v_mov_b32 v12, v252\n"
    "v_mov_b32 v13, v252\n"
    "s_nop 1\n"
    "v_permlane32_swap_b32 v12, v13\n"
    "v_rcp_f32 v12, v13\n"
    "s_cmp_eq_u32 s3, 2\n"
    "s_cbranch_scc1 .La_end\n"
    "v_mul_f32 v140, v12, v252\n"
    "v_mul_f32 v141, v12, v253\n"
    "v_mul_f32 v142, v12, v254\n"
    "v_mul_f32 v143, v12, v255\n"
    "s_mov_b32 exec_hi, 0\n"
    "v_cvt_pk_bf16_f32 v150, v140, v141\n"
    "v_cvt_pk_bf16_f32 v151, v142, v143\n"
    "s_mov_b64 exec, -1\n"
    "s_waitcnt lgkmcnt(8)\n"
    "v_add_f32 v240, v240, v192\n"
    "v_add_f32 v241, v241, v193\n"
    "v_add_f32 v242, v242, v194\n"
    "v_add_f32 v243, v243, v195\n"
    "s_waitcnt lgkmcnt(7)\n"
    "v_add_f32 v240, v240, v208\n"
    "v_add_f32 v241, v241, v209\n"
    "v_add_f32 v242, v242, v210\n"
    "v_add_f32 v243, v243, v211\n"
    "s_waitcnt lgkmcnt(6)\n"
    "v_add_f32 v240, v240, v224\n"
    "v_add_f32 v241, v241, v225\n"
    "v_add_f32 v242, v242, v226\n"
    "v_add_f32 v243, v243, v227\n"
    "v_mul_f32 v128, v12, v240\n"
    "v_mul_f32 v129, v12, v241\n"
    "v_mul_f32 v130, v12, v242\n"
    "v_mul_f32 v131, v12, v243\n"
    "v_cvt_pk_bf16_f32 v144, v128, v129\n"
    "v_cvt_pk_bf16_f32 v145, v130, v131\n"
    "s_waitcnt lgkmcnt(5)\n"
    "v_add_f32 v244, v244, v196\n"
    "v_add_f32 v245, v245, v197\n"
    "v_add_f32 v246, v246, v198\n"
    "v_add_f32 v247, v247, v199\n"
    "s_waitcnt lgkmcnt(4)\n"
    "v_add_f32 v244, v244, v212\n"
    "v_add_f32 v245, v245, v213\n"
    "v_add_f32 v246, v246, v214\n"
    "v_add_f32 v247, v247, v215\n"
    "s_waitcnt lgkmcnt(3)\n"
    "v_add_f32 v244, v244, v228\n"
    "v_add_f32 v245, v245, v229\n"
    "v_add_f32 v246, v246, v230\n"
    "v_add_f32 v247, v247, v231\n"
    "v_mul_f32 v132, v12, v244\n"
    "v_mul_f32 v133, v12, v245\n"
    "v_mul_f32 v134, v12, v246\n"
    "v_mul_f32 v135, v12, v247\n"
    "v_cvt_pk_bf16_f32 v146, v132, v133\n"
    "v_cvt_pk_bf16_f32 v147, v134, v135\n"
    "buffer_store_dwordx4 v[144:147], v2, s[16:19], s70 offen offset:0 sc1\n"
    "s_waitcnt lgkmcnt(2)\n"
    "v_add_f32 v248, v248, v200\n"
    "v_add_f32 v249, v249, v201\n"
    "v_add_f32 v250, v250, v202\n"
    "v_add_f32 v251, v251, v203\n"
    "s_waitcnt lgkmcnt(1)\n"
    "v_add_f32 v248, v248, v216\n"
    "v_add_f32 v249, v249, v217\n"
    "v_add_f32 v250, v250, v218\n"
    "v_add_f32 v251, v251, v219\n"
    "s_waitcnt lgkmcnt(0)\n"
    "v_add_f32 v248, v248, v232\n"
    "v_add_f32 v249, v249, v233\n"
    "v_add_f32 v250, v250, v234\n"
    "v_add_f32 v251, v251, v235\n"
    "v_mul_f32 v136, v12, v248\n"
    "v_mul_f32 v137, v12, v249\n"
    "v_mul_f32 v138, v12, v250\n"
    "v_mul_f32 v139, v12, v251\n"
    "v_cvt_pk_bf16_f32 v148, v136, v137\n"
    "v_cvt_pk_bf16_f32 v149, v138, v139\n"
    "buffer_store_dwordx4 v[148:151], v2, s[16:19], s70 offen offset:1024 sc1\n"
    "v_add_f32 v240, v240, v248\n"
    "v_add_f32 v241, v241, v249\n"
    "v_add_f32 v242, v242, v250\n"
    "v_add_f32 v243, v243, v251\n"
    "v_add_f32 v244, v244, v252\n"
    "v_add_f32 v245, v245, v253\n"
    "v_add_f32 v246, v246, v254\n"
    "v_add_f32 v247, v247, v255\n"
    "v_add_f32 v240, v240, v244\n"
    "v_add_f32 v241, v241, v245\n"
    "v_add_f32 v242, v242, v246\n"
    "v_add_f32 v243, v243, v247\n"
    "v_add_f32 v240, v240, v242\n"
    "v_add_f32 v241, v241, v243\n"
    "v_add_f32 v240, v240, v241\n"
    "v_fma_f32 v13, v240, 0, v13\n"
    "s_mov_b32 s54, 729808896\n"
    "v_cmp_nle_f32 s[50:51], s54, v13\n"
    "s_mov_b32 s54, 0x5d800000\n"
    "v_cmp_nge_f32 vcc, s54, v13\n"
    "s_or_b64 vcc, vcc, s[50:51]\n"
    "s_cmp_lg_u32 s3, 0\n"
    "s_cselect_b64 vcc, 0, vcc\n"
    "s_cmp_lg_u64 vcc, 0\n"
    "s_cbranch_scc1 .Lbad\n"
    "s_waitcnt vmcnt(0)\n"
    "v_mov_b64 v[18:19], s[48:49]\n"
    "s_mov_b64 exec, 1\n"
    "buffer_store_dwordx2 v[18:19], off, s[16:19], s71 sc1\n"
    "s_mov_b64 exec, -1\n"
    "s_setprio 0\n"
    "s_cmp_lg_u32 s3, 0\n"
    "s_cbranch_scc1 .La_end\n"
    "v_mov_b32 v18, 7\n"
    "ds_write_b32 v0, v18\n"
    "s_mov_b32 s3, 2\n"
    "v_mov_b32 v22, 163344\n"
    "s_mov_b32 s63, 0x100000\n"
    ".Lwp_13:\n"
    "ds_read_b128 v[18:21], v22\n"
    "s_waitcnt lgkmcnt(0)\n"
    "v_min3_u32 v23, v18, v19, v20\n"
    "v_min_u32 v23, v23, v21\n"
    "v_cmp_gt_u32 vcc, 7, v23\n"
    "s_cbranch_vccz .Lwpd_14\n"
    "s_sleep 2\n"
    "s_sub_u32 s63, s63, 1\n"
    "s_cbranch_scc0 .Lwp_13\n"
    ".Lwpd_14:\n"
    "v_max3_u32 v18, v18, v19, v20\n"
    "v_max_u32 v18, v18, v21\n"
    "v_cmp_lt_u32 vcc, 7, v18\n"
    "s_cbranch_vccnz .Lfb\n"
    ".La_end:\n"
    "s_endpgm\n"
    ".Lf_part2:\n"
    "s_and_b32 s41, s45, 3\n"
    "v_lshrrev_b32 v8, 3, v1\n"
    "v_mul_u32_u24 v8, 896, v8\n"
    "v_bfe_u32 v9, v1, 4, 1\n"
    "v_lshlrev_b32 v9, 2, v9\n"
    "v_and_b32 v10, 7, v1\n"
    "v_xor_b32 v10, v10, v9\n"
    "v_lshl_add_u32 v14, v10, 4, v8\n"
    "v_add_u32 v15, 6144, v14\n"
    "v_lshlrev_b32 v8, 5, v3\n"
    "v_xor_b32 v10, v8, v14\n"
    "v_add_u32 v11, 6144, v10\n"
    "v_bfrev_b32 v12, v1\n"
    "v_lshrrev_b32 v12, 30, v12\n"
    "v_add_u32 v12, v12, v9\n"
    "v_and_b32 v8, 8, v1\n"
    "v_xor_b32 v12, v12, v8\n"
    "v_bfe_u32 v8, v1, 2, 2\n"
    "v_lshl_add_u32 v8, v3, 2, v8\n"
    "v_lshl_add_u32 v12, v8, 4, v12\n"
    "v_lshlrev_b32 v12, 3, v12\n"
    "v_lshrrev_b32 v8, 4, v1\n"
    "v_bfe_u32 v9, v1, 2, 2\n"
    "v_lshl_add_u32 v6, v8, 2, v9\n"
    "v_and_b32 v8, 1, v8\n"
    "v_and_b32 v9, 2, v9\n"
    "v_or_b32 v8, v8, v9\n"
    "v_lshl_add_u32 v6, v6, 2, v8\n"
    "v_and_b32 v8, 3, v1\n"
    "v_lshl_add_u32 v6, v6, 2, v8\n"
    "v_lshlrev_b32 v6, 3, v6\n"
    "s_lshr_b32 s52, s45, 2\n"
    "s_mul_i32 s53, s52, 3\n"
    "s_add_u32 s53, s53, 16\n"
    "s_and_b32 s52, s45, 12\n"
    "s_add_u32 s4, s41, 0\n"
    "s_and_b32 s4, s4, 3\n"
    "s_add_u32 s4, s4, s52\n"
    "s_lshl1_add_u32 s5, s41, 0\n"
    "s_lshr_b32 s5, 0x924, s5\n"
    "s_and_b32 s5, s5, 3\n"
    "s_add_u32 s5, s5, s53\n"
    "s_add_u32 s6, s41, 1\n"
    "s_and_b32 s6, s6, 3\n"
    "s_add_u32 s6, s6, s52\n"
    "s_lshl1_add_u32 s7, s41, 2\n"
    "s_lshr_b32 s7, 0x924, s7\n"
    "s_and_b32 s7, s7, 3\n"
    "s_add_u32 s7, s7, s53\n"
    "s_add_u32 s8, s41, 2\n"
    "s_and_b32 s8, s8, 3\n"
    "s_add_u32 s8, s8, s52\n"
    "s_lshl1_add_u32 s9, s41, 4\n"
    "s_lshr_b32 s9, 0x924, s9\n"
    "s_and_b32 s9, s9, 3\n"
    "s_add_u32 s9, s9, s53\n"
    "s_add_u32 s10, s41, 3\n"
    "s_and_b32 s10, s10, 3\n"
    "s_add_u32 s10, s10, s52\n"
    "s_mul_i32 s77, s62, 56\n"
    "s_add_u32 s77, s77, 0\n"
    "s_lshl_b32 s77, s77, 11\n"
    "s_add_u32 s77, s77, 7340032\n"
    "s_lshr_b32 s52, s45, 2\n"
    "s_mul_i32 s78, s62, 14\n"
    "s_lshl2_add_u32 s78, s41, s78\n"
    "s_lshl2_add_u32 s78, s78, s52\n"
    "s_lshl_b32 s78, s78, 11\n"
    "s_add_u32 s78, s78, 16777216\n"
    "s_lshl2_add_u32 s40, s62, s41\n"
    "s_lshl2_add_u32 s40, s40, s52\n"
    "s_lshl_b32 s40, s40, 5\n"
    "s_add_u32 s40, s40, 507904\n"
    "v_and_b32 v8, 3, v1\n"
    "s_and_b32 s53, s45, 12\n"
    "v_add_u32 v9, s53, v8\n"
    "v_lshl_add_u32 v8, v8, 2, 3\n"
    "s_cmp_eq_u32 s52, 0\n"
    "s_cselect_b32 vcc_hi, -1, 0\n"
    "s_mov_b32 vcc_lo, 0\n"
    "v_cndmask_b32 v8, v9, v8, vcc\n"
    "v_lshlrev_b32 v8, 5, v8\n"
    "v_lshlrev_b32 v9, 2, v7\n"
    "s_bfe_u32 s52, s2, 0x30003\n"
    "s_and_b32 s53, s2, 1\n"
    "s_lshl2_add_u32 s53, s53, 3\n"
    "s_cmp_gt_u32 s52, s53\n"
    "s_subb_u32 s73, s52, 0\n"
    "s_lshr_b32 s53, s2, 6\n"
    "s_and_b32 s52, s2, 7\n"
    "s_lshl2_add_u32 s72, s52, s53\n"
    "s_mul_i32 s74, s47, 14336\n"
    "s_lshl_b32 s52, s73, 7\n"
    "s_mul_i32 s79, s47, 100352\n"
    "s_add_u32 s79, s79, s52\n"
    "s_and_b32 s52, s47, 1\n"
    "s_lshl_b32 s52, s52, 6\n"
    "s_add_u32 s52, s52, s74\n"
    "v_xor_b32 v13, s52, v12\n"
    "v_xor_b32 v12, 64, v13\n"
    "s_lshl_b32 s56, s41, 8\n"
    "s_add_u32 s56, s56, s74\n"
    "v_mov_b32 v228, 163344\n"
    "s_mov_b32 s63, 0x100000\n"
    ".Lwp_15:\n"
    "ds_read_b128 v[224:227], v228\n"
    "s_waitcnt lgkmcnt(0)\n"
    "v_min3_u32 v229, v224, v225, v226\n"
    "v_min_u32 v229, v229, v227\n"
    "v_cmp_gt_u32 vcc, 4, v229\n"
    "s_cbranch_vccz .Lwpd_16\n"
    "s_sleep 1\n"
    "s_sub_u32 s63, s63, 1\n"
    "s_cbranch_scc0 .Lwp_15\n"
    ".Lwpd_16:\n"
    "ds_read_b128 v[64:67], v5 offset:0\n"
    "ds_read_b128 v[68:71], v5 offset:1024\n"
    "v_mov_b32 v225, 4\n"
    "ds_write_b32 v0, v225\n"
    "s_mov_b32 s63, 0x100000\n"
    "v_mov_b32 v24, 163328\n"
    "ds_read_b128 v[16:19], v24 offset:0\n"
    "ds_read_b128 v[20:23], v24 offset:16\n"
    ".Lw2_17:\n"
    "s_waitcnt lgkmcnt(0)\n"
    "v_min3_u32 v16, v16, v17, v18\n"
    "v_min3_u32 v20, v20, v21, v22\n"
    "v_min_u32 v20, v20, v23\n"
    "v_add_u32 v20, -1, v20\n"
    "v_min3_i32 v16, v16, v19, v20\n"
    "v_cmp_gt_i32 vcc, 4, v16\n"
    "s_cbranch_vccz .Lw2d_19\n"
    "s_sleep 2\n"
    "ds_read_b128 v[16:19], v24 offset:0\n"
    "ds_read_b128 v[20:23], v24 offset:16\n"
    "s_sub_u32 s63, s63, 1\n"
    "s_cbranch_scc0 .Lw2_17\n"
    "s_waitcnt lgkmcnt(0)\n"
    ".Lw2d_19:\n"
    "s_setprio 0\n"
    "s_sleep 20\n"
    "buffer_store_dwordx4 v[64:67], v2, s[16:19], s70 offen offset:0 sc1\n"
    "s_add_u32 m0, s74, 0\n"
    "s_nop 0\n"
    "buffer_load_dwordx4 v14, s[32:35], s79 offen lds\n"
    "s_sleep 1\n"
    "s_sleep 4\n"
    "buffer_store_dwordx4 v[68:71], v2, s[16:19], s70 offen offset:1024 sc1\n"
    "buffer_load_dwordx4 v15, s[32:35], s79 offen offset:1024 lds\n"
    "s_sleep 1\n"
    "s_sleep 4\n"
    "buffer_store_dwordx4 v[72:75], v2, s[16:19], s70 offen offset:2048 sc1\n"
    "s_add_u32 m0, s74, 2048\n"
    "s_add_u32 s79, s79, 14336\n"
    "buffer_load_dwordx4 v14, s[32:35], s79 offen lds\n"
    "s_sleep 1\n"
    "s_sleep 4\n"
    "buffer_store_dwordx4 v[76:79], v2, s[16:19], s70 offen offset:3072 sc1\n"
    "buffer_load_dwordx4 v15, s[32:35], s79 offen offset:1024 lds\n"
    "s_sleep 1\n"
    "s_sleep 4\n"
    "s_cmp_le_u32 s75, 2\n"
    "s_cbranch_scc1 .Lns_20\n"
    "buffer_store_dwordx4 v[80:83], v2, s[16:19], s71 offen offset:0 sc1\n"
    ".Lns_20:\n"
    "s_add_u32 m0, s74, 4096\n"
    "s_add_u32 s79, s79, 14336\n"
    "buffer_load_dwordx4 v14, s[32:35], s79 offen lds\n"
    "s_sleep 1\n"
    "s_sleep 4\n"
    "s_cmp_le_u32 s75, 2\n"
    "s_cbranch_scc1 .Lns_21\n"
    "buffer_store_dwordx4 v[84:87], v2, s[16:19], s71 offen offset:1024 sc1\n"
    ".Lns_21:\n"
    "buffer_load_dwordx4 v15, s[32:35], s79 offen offset:1024 lds\n"
    "s_sleep 1\n"
    "s_sleep 4\n"
    "s_cmp_le_u32 s75, 3\n"
    "s_cbranch_scc1 .Lns_22\n"
    "buffer_store_dwordx4 v[88:91], v2, s[16:19], s71 offen offset:2048 sc1\n"
    ".Lns_22:\n"
    "s_add_u32 m0, s74, 6144\n"
    "s_add_u32 s79, s79, 14336\n"
    "buffer_load_dwordx4 v14, s[32:35], s79 offen lds\n"
    "s_sleep 1\n"
    "s_sleep 4\n"
    "s_cmp_le_u32 s75, 3\n"
    "s_cbranch_scc1 .Lns_23\n"
    "buffer_store_dwordx4 v[92:95], v2, s[16:19], s71 offen offset:3072 sc1\n"
    ".Lns_23:\n"
    "s_cmp_lg_u32 s45, 2\n"
    "s_cbranch_scc1 .Lf_nog\n"
    "s_lshl_b32 s52, s62, 7\n"
    "s_add_u32 s52, s52, 4096\n"
    "s_mov_b32 exec_hi, 0\n"
    "buffer_store_dword v232, v9, s[16:19], s52 offen sc1\n"
    "s_mov_b64 exec, -1\n"
    ".Lf_nog:\n"
    "buffer_load_dwordx4 v15, s[32:35], s79 offen offset:1024 lds\n"
    "s_sleep 1\n"
    "s_add_u32 m0, s74, 8192\n"
    "s_add_u32 s79, s79, 14336\n"
    "buffer_load_dwordx4 v14, s[32:35], s79 offen lds\n"
    "s_sleep 1\n"
    "buffer_load_dwordx4 v15, s[32:35], s79 offen offset:1024 lds\n"
    "s_sleep 1\n"
    "s_add_u32 m0, s74, 10240\n"
    "s_add_u32 s79, s79, 14336\n"
    "buffer_load_dwordx4 v14, s[32:35], s79 offen lds\n"
    "s_sleep 1\n"
    "buffer_load_dwordx4 v15, s[32:35], s79 offen offset:1024 lds\n"
    "s_sleep 1\n"
    "s_add_u32 m0, s74, 12288\n"
    "s_add_u32 s79, s79, 14336\n"
    "buffer_load_dwordx4 v14, s[32:35], s79 offen lds\n"
    "s_sleep 1\n"
    "buffer_load_dwordx4 v15, s[32:35], s79 offen offset:1024 lds\n"
    "s_sleep 1\n"
    "s_mov_b32 s76, 0\n"
    "s_mov_b32 s75, s30\n"
    "s_cmp_eq_u32 s41, 3\n"
    "s_cselect_b32 s30, 0, s30\n"
    "s_add_u32 m0, s55, 102400\n"
    "s_mul_i32 s54, s4, 57344\n"
    "s_add_u32 s54, s54, s56\n"
    "s_add_u32 s54, s54, 128\n"
    "buffer_load_dwordx4 v10, s[28:31], s54 offen lds\n"
    "s_sleep 1\n"
    "buffer_load_dwordx4 v11, s[28:31], s54 offen offset:1024 lds\n"
    "s_sleep 1\n"
    "s_add_u32 m0, s55, 110592\n"
    "s_mul_i32 s54, s5, 57344\n"
    "s_add_u32 s54, s54, s56\n"
    "s_add_u32 s54, s54, 128\n"
    "buffer_load_dwordx4 v10, s[28:31], s54 offen lds\n"
    "s_sleep 1\n"
    "buffer_load_dwordx4 v11, s[28:31], s54 offen offset:1024 lds\n"
    "s_sleep 1\n"
    "s_waitcnt vmcnt(11)\n"
    "s_setprio 2\n"
    "v_mov_b64 v[16:17], s[48:49]\n"
    "s_mov_b64 exec, 1\n"
    "buffer_store_dwordx2 v[16:17], off, s[16:19], s3 sc1\n"
    "s_mov_b64 exec, -1\n"
    "s_setprio 0\n"
    "s_waitcnt vmcnt(5)\n"
    "ds_read_b64_tr_b16 v[176:177], v12 offset:0\n"
    "ds_read_b64_tr_b16 v[178:179], v12 offset:1024\n"
    "ds_read_b64_tr_b16 v[180:181], v13 offset:0\n"
    "ds_read_b64_tr_b16 v[182:183], v13 offset:1024\n"
    "s_add_u32 m0, s55, 118784\n"
    "s_mul_i32 s54, s6, 57344\n"
    "s_add_u32 s54, s54, s56\n"
    "s_add_u32 s54, s54, 128\n"
    "buffer_load_dwordx4 v10, s[28:31], s54 offen lds\n"
    "s_sleep 1\n"
    "ds_read_b64_tr_b16 v[184:185], v12 offset:2048\n"
    "ds_read_b64_tr_b16 v[186:187], v12 offset:3072\n"
    "ds_read_b64_tr_b16 v[188:189], v13 offset:2048\n"
    "ds_read_b64_tr_b16 v[190:191], v13 offset:3072\n"
    "buffer_load_dwordx4 v11, s[28:31], s54 offen offset:1024 lds\n"
    "s_sleep 1\n"
    "ds_read_b64_tr_b16 v[192:193], v12 offset:3584\n"
    "ds_read_b64_tr_b16 v[194:195], v12 offset:4608\n"
    "ds_read_b64_tr_b16 v[196:197], v13 offset:3584\n"
    "ds_read_b64_tr_b16 v[198:199], v13 offset:4608\n"
    "s_add_u32 m0, s55, 126976\n"
    "s_mul_i32 s54, s7, 57344\n"
    "s_add_u32 s54, s54, s56\n"
    "s_add_u32 s54, s54, 128\n"
    "buffer_load_dwordx4 v10, s[28:31], s54 offen lds\n"
    "s_sleep 1\n"
    "ds_read_b64_tr_b16 v[200:201], v12 offset:5632\n"
    "ds_read_b64_tr_b16 v[202:203], v12 offset:6656\n"
    "ds_read_b64_tr_b16 v[204:205], v13 offset:5632\n"
    "ds_read_b64_tr_b16 v[206:207], v13 offset:6656\n"
    "buffer_load_dwordx4 v11, s[28:31], s54 offen offset:1024 lds\n"
    "s_sleep 1\n"
    "ds_read_b64_tr_b16 v[208:209], v12 offset:7168\n"
    "ds_read_b64_tr_b16 v[210:211], v12 offset:8192\n"
    "ds_read_b64_tr_b16 v[212:213], v13 offset:7168\n"
    "ds_read_b64_tr_b16 v[214:215], v13 offset:8192\n"
    "s_add_u32 m0, s55, 135168\n"
    "s_mul_i32 s54, s8, 57344\n"
    "s_add_u32 s54, s54, s56\n"
    "s_add_u32 s54, s54, 128\n"
    "buffer_load_dwordx4 v10, s[28:31], s54 offen lds\n"
    "s_sleep 1\n"
    "ds_read_b64_tr_b16 v[216:217], v12 offset:9216\n"
    "ds_read_b64_tr_b16 v[218:219], v12 offset:10240\n"
    "ds_read_b64_tr_b16 v[220:221], v13 offset:9216\n"
    "ds_read_b64_tr_b16 v[222:223], v13 offset:10240\n"
    "buffer_load_dwordx4 v11, s[28:31], s54 offen offset:1024 lds\n"
    "s_sleep 1\n"
    "ds_read_b64_tr_b16 v[224:225], v12 offset:10752\n"
    "ds_read_b64_tr_b16 v[226:227], v12 offset:11776\n"
    "ds_read_b64_tr_b16 v[228:229], v13 offset:10752\n"
    "ds_read_b64_tr_b16 v[230:231], v13 offset:11776\n"
    "s_add_u32 m0, s55, 143360\n"
    "s_mul_i32 s54, s9, 57344\n"
    "s_add_u32 s54, s54, s56\n"
    "s_add_u32 s54, s54, 128\n"
    "buffer_load_dwordx4 v10, s[28:31], s54 offen lds\n"
    "s_sleep 1\n"
    "ds_read_b64_tr_b16 v[232:233], v12 offset:12800\n"
    "ds_read_b64_tr_b16 v[234:235], v12 offset:13824\n"
    "ds_read_b64_tr_b16 v[236:237], v13 offset:12800\n"
    "ds_read_b64_tr_b16 v[238:239], v13 offset:13824\n"
    "buffer_load_dwordx4 v11, s[28:31], s54 offen offset:1024 lds\n"
    "s_sleep 1\n"
    "s_setprio 2\n"
    "v_mov_b32 v16, 5\n"
    "ds_write_b32 v0, v16\n"
    "s_setprio 0\n"
    "s_add_u32 m0, s55, 151552\n"
    "s_mul_i32 s54, s10, 57344\n"
    "s_add_u32 s54, s54, s56\n"
    "s_add_u32 s54, s54, 128\n"
    "buffer_load_dwordx4 v10, s[28:31], s54 offen lds\n"
    "s_sleep 1\n"
    "buffer_load_dwordx4 v11, s[28:31], s54 offen offset:1024 lds\n"
    "s_sleep 1\n"
    "s_mov_b32 s30, s75\n"
    "s_setprio 2\n"
    "v_mov_b32 v20, 163328\n"
    "s_mov_b32 s63, 0x100000\n"
    ".Lwp_24:\n"
    "ds_read_b128 v[16:19], v20\n"
    "s_waitcnt lgkmcnt(0)\n"
    "v_min3_u32 v21, v16, v17, v18\n"
    "v_min_u32 v21, v21, v19\n"
    "v_cmp_gt_u32 vcc, 5, v21\n"
    "s_cbranch_vccz .Lwpd_25\n"
    "s_sleep 2\n"
    "s_sub_u32 s63, s63, 1\n"
    "s_cbranch_scc0 .Lwp_24\n"
    ".Lwpd_25:\n"
    "s_setprio 0\n"
    "s_add_u32 m0, s55, 0\n"
    "s_mul_i32 s54, s4, 57344\n"
    "s_add_u32 s54, s54, s56\n"
    "buffer_load_dwordx4 v10, s[28:31], s54 offen lds\n"
    "s_sleep 1\n"
    "buffer_load_dwordx4 v11, s[28:31], s54 offen offset:1024 lds\n"
    "s_sleep 1\n"
    "s_add_u32 m0, s55, 8192\n"
    "s_mul_i32 s54, s5, 57344\n"
    "s_add_u32 s54, s54, s56\n"
    "buffer_load_dwordx4 v10, s[28:31], s54 offen lds\n"
    "s_sleep 1\n"
    "buffer_load_dwordx4 v11, s[28:31], s54 offen offset:1024 lds\n"
    "s_sleep 1\n"
    "s_add_u32 m0, s55, 16384\n"
    "s_mul_i32 s54, s6, 57344\n"
    "s_add_u32 s54, s54, s56\n"
    "buffer_load_dwordx4 v10, s[28:31], s54 offen lds\n"
    "s_sleep 1\n"
    "buffer_load_dwordx4 v11, s[28:31], s54 offen offset:1024 lds\n"
    "s_sleep 1\n"
    "s_add_u32 m0, s55, 24576\n"
    "s_mul_i32 s54, s7, 57344\n"
    "s_add_u32 s54, s54, s56\n"
    "buffer_load_dwordx4 v10, s[28:31], s54 offen lds\n"
    "s_sleep 1\n"
    "buffer_load_dwordx4 v11, s[28:31], s54 offen offset:1024 lds\n"
    "s_sleep 1\n"
    "s_add_u32 m0, s55, 32768\n"
    "s_mul_i32 s54, s8, 57344\n"
    "s_add_u32 s54, s54, s56\n"
    "buffer_load_dwordx4 v10, s[28:31], s54 offen lds\n"
    "s_sleep 1\n"
    "buffer_load_dwordx4 v11, s[28:31], s54 offen offset:1024 lds\n"
    "s_sleep 1\n"
    "s_add_u32 m0, s55, 40960\n"
    "s_mul_i32 s54, s9, 57344\n"
    "s_add_u32 s54, s54, s56\n"
    "buffer_load_dwordx4 v10, s[28:31], s54 offen lds\n"
    "s_sleep 1\n"
    "buffer_load_dwordx4 v11, s[28:31], s54 offen offset:1024 lds\n"
    "s_sleep 1\n"
    "s_add_u32 m0, s55, 49152\n"
    "s_mul_i32 s54, s10, 57344\n"
    "s_add_u32 s54, s54, s56\n"
    "buffer_load_dwordx4 v10, s[28:31], s54 offen lds\n"
    "s_sleep 1\n"
    "buffer_load_dwordx4 v11, s[28:31], s54 offen offset:1024 lds\n"
    "s_sleep 1\n"
    "s_lshl_b32 s52, s62, 9\n"
    "s_add_u32 s52, s52, 475136\n"
    "s_setprio 2\n"
    "s_mov_b32 s63, 0x20000\n"
    ".Lpf_26:\n"
    "buffer_load_dwordx2 v[16:17], v8, s[16:19], s52 offen sc1\n"
    "s_waitcnt vmcnt(0)\n"
    "v_cmp_ne_u64 vcc, s[48:49], v[16:17]\n"
    "s_cbranch_vccz .Lpfd_27\n"
    "s_sleep 16\n"
    "s_sub_u32 s63, s63, 1\n"
    "s_cbranch_scc0 .Lpf_26\n"
    ".Lpfd_27:\n"
    "v_mov_b32 v16, 6\n"
    "ds_write_b32 v0, v16\n"
    "s_or_b32 s52, s2, s47\n"
    "s_cbranch_scc1 .Lf_noe\n"
    "v_lshlrev_b32 v8, 5, v1\n"
    "s_mov_b32 s52, 403456\n"
    "s_mov_b32 s63, 0x20000\n"
    ".Lpf_28:\n"
    "buffer_load_dwordx2 v[16:17], v8, s[16:19], s52 offen sc1\n"
    "s_waitcnt vmcnt(0)\n"
    "v_cmp_ne_u64 vcc, s[48:49], v[16:17]\n"
    "s_cbranch_vccz .Lpfd_29\n"
    "s_sleep 4\n"
    "s_sub_u32 s63, s63, 1\n"
    "s_cbranch_scc0 .Lpf_28\n"
    ".Lpfd_29:\n"
    "s_mov_b32 s52, 405504\n"
    "s_mov_b32 s63, 0x20000\n"
    ".Lpf_30:\n"
    "buffer_load_dwordx2 v[16:17], v8, s[16:19], s52 offen sc1\n"
    "s_waitcnt vmcnt(0)\n"
    "v_cmp_ne_u64 vcc, s[48:49], v[16:17]\n"
    "s_cbranch_vccz .Lpfd_31\n"
    "s_sleep 4\n"
    "s_sub_u32 s63, s63, 1\n"
    "s_cbranch_scc0 .Lpf_30\n"
    ".Lpfd_31:\n"
    ".Lf_noe:\n"
    "s_setprio 0\n"
    "s_lshl_b32 s52, s6, 12\n"
    "s_add_u32 s52, s52, s77\n"
    "buffer_load_dwordx4 v[96:99], v2, s[16:19], s52 offen offset:0 sc1\n"
    "s_sleep 1\n"
    "buffer_load_dwordx4 v[100:103], v2, s[16:19], s52 offen offset:1024 sc1\n"
    "s_sleep 1\n"
    "buffer_load_dwordx4 v[104:107], v2, s[16:19], s52 offen offset:2048 sc1\n"
    "s_sleep 1\n"
    "buffer_load_dwordx4 v[108:111], v2, s[16:19], s52 offen offset:3072 sc1\n"
    "s_sleep 1\n"
    "s_setprio 2\n"
    "v_mov_b32 v20, 163328\n"
    "s_mov_b32 s63, 0x100000\n"
    ".Lwp_32:\n"
    "ds_read_b128 v[16:19], v20\n"
    "s_waitcnt lgkmcnt(0)\n"
    "v_min3_u32 v21, v16, v17, v18\n"
    "v_min_u32 v21, v21, v19\n"
    "v_cmp_gt_u32 vcc, 6, v21\n"
    "s_cbranch_vccz .Lwpd_33\n"
    "s_sleep 2\n"
    "s_sub_u32 s63, s63, 1\n"
    "s_cbranch_scc0 .Lwp_32\n"
    ".Lwpd_33:\n"
    "v_xor_b32 v12, 102400, v6\n"
    "v_xor_b32 v13, 32, v12\n"
    "v_xor_b32 v14, 64, v12\n"
    "v_xor_b32 v15, 96, v12\n"
    "ds_read_b64_tr_b16 v[32:33], v12 offset:0\n"
    "ds_read_b64_tr_b16 v[34:35], v12 offset:2048\n"
    "ds_read_b64_tr_b16 v[36:37], v13 offset:0\n"
    "ds_read_b64_tr_b16 v[38:39], v13 offset:2048\n"
    "ds_read_b64_tr_b16 v[40:41], v14 offset:0\n"
    "ds_read_b64_tr_b16 v[42:43], v14 offset:2048\n"
    "ds_read_b64_tr_b16 v[44:45], v15 offset:0\n"
    "ds_read_b64_tr_b16 v[46:47], v15 offset:2048\n"
    "ds_read_b64_tr_b16 v[48:49], v12 offset:4096\n"
    "ds_read_b64_tr_b16 v[50:51], v12 offset:6144\n"
    "ds_read_b64_tr_b16 v[52:53], v12 offset:8192\n"
    "ds_read_b64_tr_b16 v[54:55], v12 offset:10240\n"
    "v_mov_b32 v8, 7\n"
    "ds_write_b32 v0, v8\n"
    "s_setprio 0\n"
    "s_lshl_b32 s52, s7, 12\n"
    "s_add_u32 s52, s52, s77\n"
    "buffer_load_dwordx4 v[112:115], v2, s[16:19], s52 offen offset:0 sc1\n"
    "s_sleep 1\n"
    "buffer_load_dwordx4 v[116:119], v2, s[16:19], s52 offen offset:1024 sc1\n"
    "s_sleep 1\n"
    "buffer_load_dwordx4 v[120:123], v2, s[16:19], s52 offen offset:2048 sc1\n"
    "s_sleep 1\n"
    "buffer_load_dwordx4 v[124:127], v2, s[16:19], s52 offen offset:3072 sc1\n"
    "s_sleep 1\n"
    "s_lshl_b32 s52, s8, 12\n"
    "s_add_u32 s52, s52, s77\n"
    "buffer_load_dwordx4 v[128:131], v2, s[16:19], s52 offen offset:0 sc1\n"
    "s_sleep 1\n"
    "buffer_load_dwordx4 v[132:135], v2, s[16:19], s52 offen offset:1024 sc1\n"
    "s_sleep 1\n"
    "buffer_load_dwordx4 v[136:139], v2, s[16:19], s52 offen offset:2048 sc1\n"
    "s_sleep 1\n"
    "buffer_load_dwordx4 v[140:143], v2, s[16:19], s52 offen offset:3072 sc1\n"
    "s_sleep 1\n"
    "s_lshl_b32 s52, s9, 12\n"
    "s_add_u32 s52, s52, s77\n"
    "buffer_load_dwordx4 v[144:147], v2, s[16:19], s52 offen offset:0 sc1\n"
    "s_sleep 1\n"
    "buffer_load_dwordx4 v[148:151], v2, s[16:19], s52 offen offset:1024 sc1\n"
    "s_sleep 1\n"
    "buffer_load_dwordx4 v[152:155], v2, s[16:19], s52 offen offset:2048 sc1\n"
    "s_sleep 1\n"
    "buffer_load_dwordx4 v[156:159], v2, s[16:19], s52 offen offset:3072 sc1\n"
    "s_sleep 1\n"
    "s_lshl_b32 s52, s10, 12\n"
    "s_add_u32 s52, s52, s77\n"
    "buffer_load_dwordx4 v[160:163], v2, s[16:19], s52 offen offset:0 sc1\n"
    "s_sleep 1\n"
    "buffer_load_dwordx4 v[164:167], v2, s[16:19], s52 offen offset:1024 sc1\n"
    "s_sleep 1\n"
    "buffer_load_dwordx4 v[168:171], v2, s[16:19], s52 offen offset:2048 sc1\n"
    "s_sleep 1\n"
    "buffer_load_dwordx4 v[172:175], v2, s[16:19], s52 offen offset:3072 sc1\n"
    "s_sleep 1\n"
    "s_cmp_eq_u32 s67, 0\n"
    "s_cbranch_scc1 .Lf_own2\n"
    "s_lshl_b32 s52, s5, 12\n"
    "s_add_u32 s52, s52, s77\n"
    "buffer_load_dwordx4 v[80:83], v2, s[16:19], s52 offen offset:0 sc1\n"
    "s_sleep 1\n"
    "buffer_load_dwordx4 v[84:87], v2, s[16:19], s52 offen offset:1024 sc1\n"
    "s_sleep 1\n"
    "buffer_load_dwordx4 v[88:91], v2, s[16:19], s52 offen offset:2048 sc1\n"
    "s_sleep 1\n"
    "buffer_load_dwordx4 v[92:95], v2, s[16:19], s52 offen offset:3072 sc1\n"
    "s_sleep 1\n"
    ".Lf_own2:\n"
    "s_setprio 2\n"
    "s_lshr_b32 s53, s47, 1\n"
    "s_lshl_b32 s53, s53, 15\n"
    "v_add_u32 v11, s53, v2\n"
    "v_xor_b32 v10, 0x8000, v11\n"
    "v_and_b32 v4, 3, v1\n"
    "v_lshlrev_b32 v5, 5, v4\n"
    "v_lshl_add_u32 v4, v3, 4, v4\n"
    "v_lshlrev_b32 v4, 5, v4\n"
    "v_and_b32 v6, 48, v1\n"
    "v_add_u32 v6, v6, v1\n"
    "v_lshlrev_b32 v6, 4, v6\n"
    "s_add_u32 s56, s78, 24576\n"
    "s_cmp_eq_u32 s41, 3\n"
    "s_cselect_b32 s56, 0x7f000000, s56\n"
    "s_lshl3_add_u32 s53, s72, s47\n"
    "s_lshl_b32 s55, s53, 7\n"
    "s_add_u32 s55, s55, 442368\n"
    "s_lshl_b32 s77, s53, 13\n"
    "s_add_u32 s77, s77, 5242880\n"
    "s_add_u32 s76, s77, 4096\n"
    "s_waitcnt vmcnt(0)\n"
    "s_cmp_eq_u32 s41, 3\n"
    "s_cbranch_scc0 .Lf_full\n"
    "v_add_u32 v12, 4294864896, v12\n"
    "ds_read_b64_tr_b16 v[32:33], v12 offset:0\n"
    "ds_read_b64_tr_b16 v[34:35], v12 offset:2048\n"
    "ds_read_b64_tr_b16 v[36:37], v12 offset:4096\n"
    "ds_read_b64_tr_b16 v[38:39], v12 offset:6144\n"
    "ds_read_b64_tr_b16 v[40:41], v12 offset:8192\n"
    "ds_read_b64_tr_b16 v[42:43], v12 offset:10240\n"
    "ds_read_b64_tr_b16 v[44:45], v12 offset:12288\n"
    "ds_read_b64_tr_b16 v[46:47], v12 offset:14336\n"
    "ds_read_b64_tr_b16 v[48:49], v12 offset:16384\n"
    "ds_read_b64_tr_b16 v[50:51], v12 offset:18432\n"
    "ds_read_b64_tr_b16 v[52:53], v12 offset:20480\n"
    "ds_read_b64_tr_b16 v[54:55], v12 offset:22528\n"
    "s_mov_b32 s63, 0x80000\n"
    ".Lgo_34:\n"
    "ds_read_b32 v8, v0 offset:48\n"
    "s_waitcnt lgkmcnt(0)\n"
    "v_cmp_gt_u32 vcc, 4, v8\n"
    "s_cbranch_vccz .Lgod_35\n"
    "s_sleep 3\n"
    "s_sub_u32 s63, s63, 1\n"
    "s_cbranch_scc0 .Lgo_34\n"
    ".Lgod_35:\n"
    "s_branch .Lf_half\n"
    ".Lf_full:\n"
    "s_mov_b32 s63, 0x80000\n"
    ".Lgo_36:\n"
    "ds_read_b32 v8, v0 offset:48\n"
    "s_waitcnt lgkmcnt(0)\n"
    "v_cmp_gt_u32 vcc, 4, v8\n"
    "s_cbranch_vccz .Lgod_37\n"
    "s_sleep 3\n"
    "s_sub_u32 s63, s63, 1\n"
    "s_cbranch_scc0 .Lgo_36\n"
    ".Lgod_37:\n"
    "v_mfma_f32_16x16x32_bf16 v[16:19], v[32:35], v[64:67], 0\n"
    "ds_read_b64_tr_b16 v[56:57], v12 offset:12288\n"
    "v_mfma_f32_16x16x32_bf16 v[24:27], v[32:35], v[68:71], 0\n"
    "ds_read_b64_tr_b16 v[58:59], v12 offset:14336\n"
    "v_mfma_f32_16x16x32_bf16 v[28:31], v[36:39], v[68:71], 0\n"
    "ds_read_b64_tr_b16 v[60:61], v12 offset:16384\n"
    "v_mfma_f32_16x16x32_bf16 v[20:23], v[36:39], v[64:67], 0\n"
    "ds_read_b64_tr_b16 v[62:63], v12 offset:18432\n"
    "v_mfma_f32_16x16x32_bf16 v[240:243], v[40:43], v[64:67], 0\n"
    "ds_read_b64_tr_b16 v[32:33], v12 offset:20480\n"
    "v_mfma_f32_16x16x32_bf16 v[248:251], v[40:43], v[68:71], 0\n"
    "ds_read_b64_tr_b16 v[34:35], v12 offset:22528\n"
    "v_mfma_f32_16x16x32_bf16 v[252:255], v[44:47], v[68:71], 0\n"
    "ds_read_b64_tr_b16 v[36:37], v12 offset:24576\n"
    "v_mfma_f32_16x16x32_bf16 v[244:247], v[44:47], v[64:67], 0\n"
    "ds_read_b64_tr_b16 v[38:39], v12 offset:26624\n"
    "v_mfma_f32_16x16x32_bf16 v[16:19], v[48:51], v[72:75], v[16:19]\n"
    "ds_read_b64_tr_b16 v[40:41], v12 offset:28672\n"
    "v_mfma_f32_16x16x32_bf16 v[16:19], v[52:55], v[80:83], v[16:19]\n"
    "ds_read_b64_tr_b16 v[42:43], v12 offset:30720\n"
    "v_mfma_f32_16x16x32_bf16 v[24:27], v[48:51], v[76:79], v[24:27]\n"
    "ds_read_b64_tr_b16 v[44:45], v12 offset:32768\n"
    "v_mfma_f32_16x16x32_bf16 v[24:27], v[52:55], v[84:87], v[24:27]\n"
    "ds_read_b64_tr_b16 v[46:47], v12 offset:34816\n"
    "s_waitcnt lgkmcnt(8)\n"
    "v_mfma_f32_16x16x32_bf16 v[24:27], v[56:59], v[92:95], v[24:27]\n"
    "ds_read_b64_tr_b16 v[48:49], v12 offset:36864\n"
    "v_mfma_f32_16x16x32_bf16 v[24:27], v[60:63], v[100:103], v[24:27]\n"
    "ds_read_b64_tr_b16 v[50:51], v12 offset:38912\n"
    "v_mfma_f32_16x16x32_bf16 v[16:19], v[56:59], v[88:91], v[16:19]\n"
    "ds_read_b64_tr_b16 v[52:53], v12 offset:40960\n"
    "v_mfma_f32_16x16x32_bf16 v[16:19], v[60:63], v[96:99], v[16:19]\n"
    "ds_read_b64_tr_b16 v[54:55], v12 offset:43008\n"
    "s_waitcnt lgkmcnt(8)\n"
    "v_mfma_f32_16x16x32_bf16 v[16:19], v[32:35], v[104:107], v[16:19]\n"
    "ds_read_b64_tr_b16 v[56:57], v12 offset:45056\n"
    "v_mfma_f32_16x16x32_bf16 v[16:19], v[36:39], v[112:115], v[16:19]\n"
    "ds_read_b64_tr_b16 v[58:59], v12 offset:47104\n"
    "v_mfma_f32_16x16x32_bf16 v[24:27], v[32:35], v[108:111], v[24:27]\n"
    "ds_read_b64_tr_b16 v[60:61], v12 offset:49152\n"
    "v_mfma_f32_16x16x32_bf16 v[24:27], v[36:39], v[116:119], v[24:27]\n"
    "ds_read_b64_tr_b16 v[62:63], v12 offset:51200\n"
    "s_waitcnt lgkmcnt(8)\n"
    "v_mfma_f32_16x16x32_bf16 v[24:27], v[40:43], v[124:127], v[24:27]\n"
    "ds_read_b64_tr_b16 v[32:33], v12 offset:53248\n"
    "v_mfma_f32_16x16x32_bf16 v[24:27], v[44:47], v[132:135], v[24:27]\n"
    "ds_read_b64_tr_b16 v[34:35], v12 offset:55296\n"
    "v_mfma_f32_16x16x32_bf16 v[16:19], v[40:43], v[120:123], v[16:19]\n"
    "ds_read_b64_tr_b16 v[36:37], v13 offset:4096\n"
    "v_mfma_f32_16x16x32_bf16 v[16:19], v[44:47], v[128:131], v[16:19]\n"
    "ds_read_b64_tr_b16 v[38:39], v13 offset:6144\n"
    "s_waitcnt lgkmcnt(8)\n"
    "v_mfma_f32_16x16x32_bf16 v[16:19], v[48:51], v[136:139], v[16:19]\n"
    "ds_read_b64_tr_b16 v[40:41], v13 offset:8192\n"
    "v_mfma_f32_16x16x32_bf16 v[16:19], v[52:55], v[144:147], v[16:19]\n"
    "ds_read_b64_tr_b16 v[42:43], v13 offset:10240\n"
    "v_mfma_f32_16x16x32_bf16 v[24:27], v[48:51], v[140:143], v[24:27]\n"
    "ds_read_b64_tr_b16 v[44:45], v13 offset:12288\n"
    "v_mfma_f32_16x16x32_bf16 v[24:27], v[52:55], v[148:151], v[24:27]\n"
    "ds_read_b64_tr_b16 v[46:47], v13 offset:14336\n"
    "s_waitcnt lgkmcnt(8)\n"
    "v_mfma_f32_16x16x32_bf16 v[24:27], v[56:59], v[156:159], v[24:27]\n"
    "ds_read_b64_tr_b16 v[48:49], v13 offset:16384\n"
    "v_mfma_f32_16x16x32_bf16 v[24:27], v[60:63], v[164:167], v[24:27]\n"
    "ds_read_b64_tr_b16 v[50:51], v13 offset:18432\n"
    "v_mfma_f32_16x16x32_bf16 v[16:19], v[56:59], v[152:155], v[16:19]\n"
    "ds_read_b64_tr_b16 v[52:53], v13 offset:20480\n"
    "v_mfma_f32_16x16x32_bf16 v[16:19], v[60:63], v[160:163], v[16:19]\n"
    "ds_read_b64_tr_b16 v[54:55], v13 offset:22528\n"
    "s_waitcnt lgkmcnt(10)\n"
    "v_mfma_f32_16x16x32_bf16 v[16:19], v[32:35], v[168:171], v[16:19]\n"
    "ds_read_b64_tr_b16 v[56:57], v13 offset:24576\n"
    "v_mfma_f32_16x16x32_bf16 v[24:27], v[32:35], v[172:175], v[24:27]\n"
    "ds_read_b64_tr_b16 v[58:59], v13 offset:26624\n"
    "s_waitcnt lgkmcnt(8)\n"
    "v_mfma_f32_16x16x32_bf16 v[28:31], v[36:39], v[76:79], v[28:31]\n"
    "ds_read_b64_tr_b16 v[60:61], v13 offset:28672\n"
    "v_mfma_f32_16x16x32_bf16 v[28:31], v[40:43], v[84:87], v[28:31]\n"
    "ds_read_b64_tr_b16 v[62:63], v13 offset:30720\n"
    "v_mfma_f32_16x16x32_bf16 v[20:23], v[36:39], v[72:75], v[20:23]\n"
    "ds_read_b64_tr_b16 v[32:33], v13 offset:32768\n"
    "v_mfma_f32_16x16x32_bf16 v[20:23], v[40:43], v[80:83], v[20:23]\n"
    "ds_read_b64_tr_b16 v[34:35], v13 offset:34816\n"
    "s_waitcnt lgkmcnt(8)\n"
    "v_mfma_f32_16x16x32_bf16 v[20:23], v[44:47], v[88:91], v[20:23]\n"
    "ds_read_b64_tr_b16 v[36:37], v13 offset:36864\n"
    "v_mfma_f32_16x16x32_bf16 v[20:23], v[48:51], v[96:99], v[20:23]\n"
    "ds_read_b64_tr_b16 v[38:39], v13 offset:38912\n"
    "v_mfma_f32_16x16x32_bf16 v[28:31], v[44:47], v[92:95], v[28:31]\n"
    "ds_read_b64_tr_b16 v[40:41], v13 offset:40960\n"
    "v_mfma_f32_16x16x32_bf16 v[28:31], v[48:51], v[100:103], v[28:31]\n"
    "ds_read_b64_tr_b16 v[42:43], v13 offset:43008\n"
    "s_waitcnt lgkmcnt(8)\n"
    "v_mfma_f32_16x16x32_bf16 v[28:31], v[52:55], v[108:111], v[28:31]\n"
    "ds_read_b64_tr_b16 v[44:45], v13 offset:45056\n"
    "v_mfma_f32_16x16x32_bf16 v[28:31], v[56:59], v[116:119], v[28:31]\n"
    "ds_read_b64_tr_b16 v[46:47], v13 offset:47104\n"
    "v_mfma_f32_16x16x32_bf16 v[20:23], v[52:55], v[104:107], v[20:23]\n"
    "ds_read_b64_tr_b16 v[48:49], v13 offset:49152\n"
    "v_mfma_f32_16x16x32_bf16 v[20:23], v[56:59], v[112:115], v[20:23]\n"
    "ds_read_b64_tr_b16 v[50:51], v13 offset:51200\n"
    "s_waitcnt lgkmcnt(8)\n"
    "v_mfma_f32_16x16x32_bf16 v[20:23], v[60:63], v[120:123], v[20:23]\n"
    "ds_read_b64_tr_b16 v[52:53], v13 offset:53248\n"
    "v_mfma_f32_16x16x32_bf16 v[20:23], v[32:35], v[128:131], v[20:23]\n"
    "ds_read_b64_tr_b16 v[54:55], v13 offset:55296\n"
    "v_mfma_f32_16x16x32_bf16 v[28:31], v[60:63], v[124:127], v[28:31]\n"
    "ds_read_b64_tr_b16 v[56:57], v14 offset:4096\n"
    "v_mfma_f32_16x16x32_bf16 v[28:31], v[32:35], v[132:135], v[28:31]\n"
    "ds_read_b64_tr_b16 v[58:59], v14 offset:6144\n"
    "s_waitcnt lgkmcnt(8)\n"
    "v_mfma_f32_16x16x32_bf16 v[28:31], v[36:39], v[140:143], v[28:31]\n"
    "ds_read_b64_tr_b16 v[60:61], v14 offset:8192\n"
    "v_mfma_f32_16x16x32_bf16 v[28:31], v[40:43], v[148:151], v[28:31]\n"
    "ds_read_b64_tr_b16 v[62:63], v14 offset:10240\n"
    "v_mfma_f32_16x16x32_bf16 v[20:23], v[36:39], v[136:139], v[20:23]\n"
    "ds_read_b64_tr_b16 v[32:33], v14 offset:12288\n"
    "v_mfma_f32_16x16x32_bf16 v[20:23], v[40:43], v[144:147], v[20:23]\n"
    "ds_read_b64_tr_b16 v[34:35], v14 offset:14336\n"
    "s_waitcnt lgkmcnt(8)\n"
    "v_mfma_f32_16x16x32_bf16 v[20:23], v[44:47], v[152:155], v[20:23]\n"
    "ds_read_b64_tr_b16 v[36:37], v14 offset:16384\n"
    "v_mfma_f32_16x16x32_bf16 v[20:23], v[48:51], v[160:163], v[20:23]\n"
    "ds_read_b64_tr_b16 v[38:39], v14 offset:18432\n"
    "v_mfma_f32_16x16x32_bf16 v[28:31], v[44:47], v[156:159], v[28:31]\n"
    "ds_read_b64_tr_b16 v[40:41], v14 offset:20480\n"
    "v_mfma_f32_16x16x32_bf16 v[28:31], v[48:51], v[164:167], v[28:31]\n"
    "ds_read_b64_tr_b16 v[42:43], v14 offset:22528\n"
    "s_waitcnt lgkmcnt(10)\n"
    "v_mfma_f32_16x16x32_bf16 v[28:31], v[52:55], v[172:175], v[28:31]\n"
    "ds_read_b64_tr_b16 v[44:45], v14 offset:24576\n"
    "v_mfma_f32_16x16x32_bf16 v[20:23], v[52:55], v[168:171], v[20:23]\n"
    "ds_read_b64_tr_b16 v[46:47], v14 offset:26624\n"
    "s_waitcnt lgkmcnt(8)\n"
    "v_mfma_f32_16x16x32_bf16 v[240:243], v[56:59], v[72:75], v[240:243]\n"
    "ds_read_b64_tr_b16 v[48:49], v14 offset:28672\n"
    "v_mfma_f32_16x16x32_bf16 v[240:243], v[60:63], v[80:83], v[240:243]\n"
    "ds_read_b64_tr_b16 v[50:51], v14 offset:30720\n"
    "v_mfma_f32_16x16x32_bf16 v[248:251], v[56:59], v[76:79], v[248:251]\n"
    "ds_read_b64_tr_b16 v[52:53], v14 offset:32768\n"
    "v_mfma_f32_16x16x32_bf16 v[248:251], v[60:63], v[84:87], v[248:251]\n"
    "ds_read_b64_tr_b16 v[54:55], v14 offset:34816\n"
    "s_waitcnt lgkmcnt(8)\n"
    "v_mfma_f32_16x16x32_bf16 v[248:251], v[32:35], v[92:95], v[248:251]\n"
    "ds_read_b64_tr_b16 v[56:57], v14 offset:36864\n"
    "v_mfma_f32_16x16x32_bf16 v[248:251], v[36:39], v[100:103], v[248:251]\n"
    "ds_read_b64_tr_b16 v[58:59], v14 offset:38912\n"
    "v_mfma_f32_16x16x32_bf16 v[240:243], v[32:35], v[88:91], v[240:243]\n"
    "ds_read_b64_tr_b16 v[60:61], v14 offset:40960\n"
    "v_mfma_f32_16x16x32_bf16 v[240:243], v[36:39], v[96:99], v[240:243]\n"
    "ds_read_b64_tr_b16 v[62:63], v14 offset:43008\n"
    "v_cvt_pk_bf16_f32 v16, v16, v17\n"
    "v_cvt_pk_bf16_f32 v17, v18, v19\n"
    "s_waitcnt lgkmcnt(8)\n"
    "v_mfma_f32_16x16x32_bf16 v[240:243], v[40:43], v[104:107], v[240:243]\n"
    "ds_read_b64_tr_b16 v[32:33], v14 offset:45056\n"
    "v_mfma_f32_16x16x32_bf16 v[240:243], v[44:47], v[112:115], v[240:243]\n"
    "ds_read_b64_tr_b16 v[34:35], v14 offset:47104\n"
    "v_cvt_pk_bf16_f32 v18, v20, v21\n"
    "v_cvt_pk_bf16_f32 v19, v22, v23\n"
    "v_mfma_f32_16x16x32_bf16 v[248:251], v[40:43], v[108:111], v[248:251]\n"
    "ds_read_b64_tr_b16 v[36:37], v14 offset:49152\n"
    "v_mfma_f32_16x16x32_bf16 v[248:251], v[44:47], v[116:119], v[248:251]\n"
    "ds_read_b64_tr_b16 v[38:39], v14 offset:51200\n"
    "v_cvt_pk_bf16_f32 v20, v24, v25\n"
    "v_cvt_pk_bf16_f32 v21, v26, v27\n"
    "s_waitcnt lgkmcnt(8)\n"
    "v_mfma_f32_16x16x32_bf16 v[248:251], v[48:51], v[124:127], v[248:251]\n"
    "ds_read_b64_tr_b16 v[40:41], v14 offset:53248\n"
    "v_mfma_f32_16x16x32_bf16 v[248:251], v[52:55], v[132:135], v[248:251]\n"
    "ds_read_b64_tr_b16 v[42:43], v14 offset:55296\n"
    "v_cvt_pk_bf16_f32 v22, v28, v29\n"
    "v_cvt_pk_bf16_f32 v23, v30, v31\n"
    "v_mfma_f32_16x16x32_bf16 v[240:243], v[48:51], v[120:123], v[240:243]\n"
    "ds_read_b64_tr_b16 v[44:45], v15 offset:4096\n"
    "v_mfma_f32_16x16x32_bf16 v[240:243], v[52:55], v[128:131], v[240:243]\n"
    "ds_read_b64_tr_b16 v[46:47], v15 offset:6144\n"
    "s_add_u32 s53, s78, 16384\n"
    "buffer_store_dwordx4 v[16:19], v6, s[16:19], s53 offen offset:0 sc1\n"
    "s_waitcnt lgkmcnt(8)\n"
    "v_mfma_f32_16x16x32_bf16 v[240:243], v[56:59], v[136:139], v[240:243]\n"
    "ds_read_b64_tr_b16 v[48:49], v15 offset:8192\n"
    "v_mfma_f32_16x16x32_bf16 v[240:243], v[60:63], v[144:147], v[240:243]\n"
    "ds_read_b64_tr_b16 v[50:51], v15 offset:10240\n"
    "buffer_store_dwordx4 v[20:23], v6, s[16:19], s53 offen offset:256 sc1\n"
    "v_mfma_f32_16x16x32_bf16 v[248:251], v[56:59], v[140:143], v[248:251]\n"
    "ds_read_b64_tr_b16 v[52:53], v15 offset:12288\n"
    "v_mfma_f32_16x16x32_bf16 v[248:251], v[60:63], v[148:151], v[248:251]\n"
    "ds_read_b64_tr_b16 v[54:55], v15 offset:14336\n"
    "s_waitcnt lgkmcnt(8)\n"
    "v_mfma_f32_16x16x32_bf16 v[248:251], v[32:35], v[156:159], v[248:251]\n"
    "ds_read_b64_tr_b16 v[56:57], v15 offset:16384\n"
    "v_mfma_f32_16x16x32_bf16 v[248:251], v[36:39], v[164:167], v[248:251]\n"
    "ds_read_b64_tr_b16 v[58:59], v15 offset:18432\n"
    "v_mfma_f32_16x16x32_bf16 v[240:243], v[32:35], v[152:155], v[240:243]\n"
    "ds_read_b64_tr_b16 v[60:61], v15 offset:20480\n"
    "v_mfma_f32_16x16x32_bf16 v[240:243], v[36:39], v[160:163], v[240:243]\n"
    "ds_read_b64_tr_b16 v[62:63], v15 offset:22528\n"
    "s_waitcnt lgkmcnt(10)\n"
    "v_mfma_f32_16x16x32_bf16 v[240:243], v[40:43], v[168:171], v[240:243]\n"
    "ds_read_b64_tr_b16 v[32:33], v15 offset:24576\n"
    "v_mfma_f32_16x16x32_bf16 v[248:251], v[40:43], v[172:175], v[248:251]\n"
    "ds_read_b64_tr_b16 v[34:35], v15 offset:26624\n"
    "s_waitcnt lgkmcnt(8)\n"
    "v_mfma_f32_16x16x32_bf16 v[252:255], v[44:47], v[76:79], v[252:255]\n"
    "ds_read_b64_tr_b16 v[36:37], v15 offset:28672\n"
    "v_mfma_f32_16x16x32_bf16 v[252:255], v[48:51], v[84:87], v[252:255]\n"
    "ds_read_b64_tr_b16 v[38:39], v15 offset:30720\n"
    "v_mfma_f32_16x16x32_bf16 v[244:247], v[44:47], v[72:75], v[244:247]\n"
    "ds_read_b64_tr_b16 v[40:41], v15 offset:32768\n"
    "v_mfma_f32_16x16x32_bf16 v[244:247], v[48:51], v[80:83], v[244:247]\n"
    "ds_read_b64_tr_b16 v[42:43], v15 offset:34816\n"
    "s_waitcnt lgkmcnt(8)\n"
    "v_mfma_f32_16x16x32_bf16 v[244:247], v[52:55], v[88:91], v[244:247]\n"
    "ds_read_b64_tr_b16 v[44:45], v15 offset:36864\n"
    "v_mfma_f32_16x16x32_bf16 v[244:247], v[56:59], v[96:99], v[244:247]\n"
    "ds_read_b64_tr_b16 v[46:47], v15 offset:38912\n"
    "v_mfma_f32_16x16x32_bf16 v[252:255], v[52:55], v[92:95], v[252:255]\n"
    "ds_read_b64_tr_b16 v[48:49], v15 offset:40960\n"
    "v_mfma_f32_16x16x32_bf16 v[252:255], v[56:59], v[100:103], v[252:255]\n"
    "ds_read_b64_tr_b16 v[50:51], v15 offset:43008\n"
    "s_waitcnt lgkmcnt(8)\n"
    "v_mfma_f32_16x16x32_bf16 v[252:255], v[60:63], v[108:111], v[252:255]\n"
    "ds_read_b64_tr_b16 v[52:53], v15 offset:45056\n"
    "v_mfma_f32_16x16x32_bf16 v[252:255], v[32:35], v[116:119], v[252:255]\n"
    "ds_read_b64_tr_b16 v[54:55], v15 offset:47104\n"
    "v_mfma_f32_16x16x32_bf16 v[244:247], v[60:63], v[104:107], v[244:247]\n"
    "ds_read_b64_tr_b16 v[56:57], v15 offset:49152\n"
    "v_mfma_f32_16x16x32_bf16 v[244:247], v[32:35], v[112:115], v[244:247]\n"
    "ds_read_b64_tr_b16 v[58:59], v15 offset:51200\n"
    "s_waitcnt lgkmcnt(8)\n"
    "v_mfma_f32_16x16x32_bf16 v[244:247], v[36:39], v[120:123], v[244:247]\n"
    "ds_read_b64_tr_b16 v[60:61], v15 offset:53248\n"
    "v_mfma_f32_16x16x32_bf16 v[244:247], v[40:43], v[128:131], v[244:247]\n"
    "ds_read_b64_tr_b16 v[62:63], v15 offset:55296\n"
    "v_mfma_f32_16x16x32_bf16 v[252:255], v[36:39], v[124:127], v[252:255]\n"
    "v_add_u32 v12, 4294864896, v12\n"
    "ds_read_b64_tr_b16 v[32:33], v12 offset:0\n"
    "v_mfma_f32_16x16x32_bf16 v[252:255], v[40:43], v[132:135], v[252:255]\n"
    "ds_read_b64_tr_b16 v[34:35], v12 offset:2048\n"
    "s_waitcnt lgkmcnt(8)\n"
    "v_mfma_f32_16x16x32_bf16 v[252:255], v[44:47], v[140:143], v[252:255]\n"
    "ds_read_b64_tr_b16 v[36:37], v12 offset:4096\n"
    "v_mfma_f32_16x16x32_bf16 v[252:255], v[48:51], v[148:151], v[252:255]\n"
    "ds_read_b64_tr_b16 v[38:39], v12 offset:6144\n"
    "v_mfma_f32_16x16x32_bf16 v[244:247], v[44:47], v[136:139], v[244:247]\n"
    "ds_read_b64_tr_b16 v[40:41], v12 offset:8192\n"
    "v_mfma_f32_16x16x32_bf16 v[244:247], v[48:51], v[144:147], v[244:247]\n"
    "ds_read_b64_tr_b16 v[42:43], v12 offset:10240\n"
    "s_waitcnt lgkmcnt(8)\n"
    "v_mfma_f32_16x16x32_bf16 v[244:247], v[52:55], v[152:155], v[244:247]\n"
    "ds_read_b64_tr_b16 v[44:45], v12 offset:12288\n"
    "v_mfma_f32_16x16x32_bf16 v[244:247], v[56:59], v[160:163], v[244:247]\n"
    "ds_read_b64_tr_b16 v[46:47], v12 offset:14336\n"
    "v_mfma_f32_16x16x32_bf16 v[252:255], v[52:55], v[156:159], v[252:255]\n"
    "ds_read_b64_tr_b16 v[48:49], v12 offset:16384\n"
    "v_mfma_f32_16x16x32_bf16 v[252:255], v[56:59], v[164:167], v[252:255]\n"
    "ds_read_b64_tr_b16 v[50:51], v12 offset:18432\n"
    "s_waitcnt lgkmcnt(10)\n"
    "v_mfma_f32_16x16x32_bf16 v[252:255], v[60:63], v[172:175], v[252:255]\n"
    "ds_read_b64_tr_b16 v[52:53], v12 offset:20480\n"
    "v_mfma_f32_16x16x32_bf16 v[244:247], v[60:63], v[168:171], v[244:247]\n"
    "ds_read_b64_tr_b16 v[54:55], v12 offset:22528\n"
    ".Lf_half:\n"
    "s_waitcnt lgkmcnt(8)\n"
    "v_mfma_f32_16x16x32_bf16 v[16:19], v[32:35], v[64:67], 0\n"
    "ds_read_b64_tr_b16 v[56:57], v12 offset:24576\n"
    "v_mfma_f32_16x16x32_bf16 v[16:19], v[36:39], v[72:75], v[16:19]\n"
    "ds_read_b64_tr_b16 v[58:59], v12 offset:26624\n"
    "v_mfma_f32_16x16x32_bf16 v[24:27], v[32:35], v[68:71], 0\n"
    "ds_read_b64_tr_b16 v[60:61], v12 offset:28672\n"
    "v_mfma_f32_16x16x32_bf16 v[24:27], v[36:39], v[76:79], v[24:27]\n"
    "ds_read_b64_tr_b16 v[62:63], v12 offset:30720\n"
    "s_waitcnt lgkmcnt(8)\n"
    "v_mfma_f32_16x16x32_bf16 v[24:27], v[40:43], v[84:87], v[24:27]\n"
    "ds_read_b64_tr_b16 v[32:33], v12 offset:32768\n"
    "v_mfma_f32_16x16x32_bf16 v[24:27], v[44:47], v[92:95], v[24:27]\n"
    "ds_read_b64_tr_b16 v[34:35], v12 offset:34816\n"
    "v_mov_b32 v8, 9\n"
    "ds_write_b32 v0, v8\n"
    "v_mfma_f32_16x16x32_bf16 v[16:19], v[40:43], v[80:83], v[16:19]\n"
    "ds_read_b64_tr_b16 v[36:37], v12 offset:36864\n"
    "v_mfma_f32_16x16x32_bf16 v[16:19], v[44:47], v[88:91], v[16:19]\n"
    "ds_read_b64_tr_b16 v[38:39], v12 offset:38912\n"
    "v_cvt_pk_bf16_f32 v240, v240, v241\n"
    "v_cvt_pk_bf16_f32 v241, v242, v243\n"
    "s_waitcnt lgkmcnt(9)\n"
    "v_mfma_f32_16x16x32_bf16 v[16:19], v[48:51], v[96:99], v[16:19]\n"
    "ds_read_b64_tr_b16 v[40:41], v12 offset:40960\n"
    "v_mfma_f32_16x16x32_bf16 v[16:19], v[52:55], v[104:107], v[16:19]\n"
    "ds_read_b64_tr_b16 v[42:43], v12 offset:43008\n"
    "v_cvt_pk_bf16_f32 v242, v244, v245\n"
    "v_cvt_pk_bf16_f32 v243, v246, v247\n"
    "v_mfma_f32_16x16x32_bf16 v[24:27], v[48:51], v[100:103], v[24:27]\n"
    "ds_read_b64_tr_b16 v[44:45], v12 offset:45056\n"
    "v_mfma_f32_16x16x32_bf16 v[24:27], v[52:55], v[108:111], v[24:27]\n"
    "ds_read_b64_tr_b16 v[46:47], v12 offset:47104\n"
    "v_cvt_pk_bf16_f32 v244, v248, v249\n"
    "v_cvt_pk_bf16_f32 v245, v250, v251\n"
    "s_waitcnt lgkmcnt(9)\n"
    "v_mfma_f32_16x16x32_bf16 v[24:27], v[56:59], v[116:119], v[24:27]\n"
    "ds_read_b64_tr_b16 v[48:49], v12 offset:49152\n"
    "v_mfma_f32_16x16x32_bf16 v[24:27], v[60:63], v[124:127], v[24:27]\n"
    "ds_read_b64_tr_b16 v[50:51], v12 offset:51200\n"
    "v_cvt_pk_bf16_f32 v246, v252, v253\n"
    "v_cvt_pk_bf16_f32 v247, v254, v255\n"
    "v_mfma_f32_16x16x32_bf16 v[16:19], v[56:59], v[112:115], v[16:19]\n"
    "ds_read_b64_tr_b16 v[52:53], v12 offset:53248\n"
    "v_mfma_f32_16x16x32_bf16 v[16:19], v[60:63], v[120:123], v[16:19]\n"
    "ds_read_b64_tr_b16 v[54:55], v12 offset:55296\n"
    "buffer_store_dwordx4 v[240:243], v6, s[16:19], s56 offen offset:0 sc1\n"
    "buffer_store_dwordx4 v[244:247], v6, s[16:19], s56 offen offset:256 sc1\n"
    "s_waitcnt lgkmcnt(8)\n"
    "v_mfma_f32_16x16x32_bf16 v[16:19], v[32:35], v[128:131], v[16:19]\n"
    "v_add_u32 v13, 4294864896, v13\n"
    "ds_read_b64_tr_b16 v[56:57], v13 offset:0\n"
    "v_mfma_f32_16x16x32_bf16 v[16:19], v[36:39], v[136:139], v[16:19]\n"
    "ds_read_b64_tr_b16 v[58:59], v13 offset:2048\n"
    "v_mfma_f32_16x16x32_bf16 v[24:27], v[32:35], v[132:135], v[24:27]\n"
    "ds_read_b64_tr_b16 v[60:61], v13 offset:4096\n"
    "v_mfma_f32_16x16x32_bf16 v[24:27], v[36:39], v[140:143], v[24:27]\n"
    "ds_read_b64_tr_b16 v[62:63], v13 offset:6144\n"
    "s_waitcnt lgkmcnt(8)\n"
    "v_mfma_f32_16x16x32_bf16 v[24:27], v[40:43], v[148:151], v[24:27]\n"
    "ds_read_b64_tr_b16 v[32:33], v13 offset:8192\n"
    "v_mfma_f32_16x16x32_bf16 v[24:27], v[44:47], v[156:159], v[24:27]\n"
    "ds_read_b64_tr_b16 v[34:35], v13 offset:10240\n"
    "v_mfma_f32_16x16x32_bf16 v[16:19], v[40:43], v[144:147], v[16:19]\n"
    "ds_read_b64_tr_b16 v[36:37], v13 offset:12288\n"
    "v_mfma_f32_16x16x32_bf16 v[16:19], v[44:47], v[152:155], v[16:19]\n"
    "ds_read_b64_tr_b16 v[38:39], v13 offset:14336\n"
    "s_waitcnt lgkmcnt(8)\n"
    "v_mfma_f32_16x16x32_bf16 v[16:19], v[48:51], v[160:163], v[16:19]\n"
    "ds_read_b64_tr_b16 v[40:41], v13 offset:16384\n"
    "v_mfma_f32_16x16x32_bf16 v[16:19], v[52:55], v[168:171], v[16:19]\n"
    "ds_read_b64_tr_b16 v[42:43], v13 offset:18432\n"
    "v_mfma_f32_16x16x32_bf16 v[24:27], v[48:51], v[164:167], v[24:27]\n"
    "ds_read_b64_tr_b16 v[44:45], v13 offset:20480\n"
    "v_mfma_f32_16x16x32_bf16 v[24:27], v[52:55], v[172:175], v[24:27]\n"
    "ds_read_b64_tr_b16 v[46:47], v13 offset:22528\n"
    "s_waitcnt lgkmcnt(8)\n"
    "v_mfma_f32_16x16x32_bf16 v[28:31], v[56:59], v[68:71], 0\n"
    "ds_read_b64_tr_b16 v[48:49], v13 offset:24576\n"
    "v_mfma_f32_16x16x32_bf16 v[28:31], v[60:63], v[76:79], v[28:31]\n"
    "ds_read_b64_tr_b16 v[50:51], v13 offset:26624\n"
    "v_mfma_f32_16x16x32_bf16 v[20:23], v[56:59], v[64:67], 0\n"
    "ds_read_b64_tr_b16 v[52:53], v13 offset:28672\n"
    "v_mfma_f32_16x16x32_bf16 v[20:23], v[60:63], v[72:75], v[20:23]\n"
    "ds_read_b64_tr_b16 v[54:55], v13 offset:30720\n"
    "s_waitcnt lgkmcnt(8)\n"
    "v_mfma_f32_16x16x32_bf16 v[20:23], v[32:35], v[80:83], v[20:23]\n"
    "ds_read_b64_tr_b16 v[56:57], v13 offset:32768\n"
    "v_mfma_f32_16x16x32_bf16 v[20:23], v[36:39], v[88:91], v[20:23]\n"
    "ds_read_b64_tr_b16 v[58:59], v13 offset:34816\n"
    "v_mfma_f32_16x16x32_bf16 v[28:31], v[32:35], v[84:87], v[28:31]\n"
    "ds_read_b64_tr_b16 v[60:61], v13 offset:36864\n"
    "v_mfma_f32_16x16x32_bf16 v[28:31], v[36:39], v[92:95], v[28:31]\n"
    "ds_read_b64_tr_b16 v[62:63], v13 offset:38912\n"
    "s_waitcnt lgkmcnt(8)\n"
    "v_mfma_f32_16x16x32_bf16 v[28:31], v[40:43], v[100:103], v[28:31]\n"
    "ds_read_b64_tr_b16 v[32:33], v13 offset:40960\n"
    "v_mfma_f32_16x16x32_bf16 v[28:31], v[44:47], v[108:111], v[28:31]\n"
    "ds_read_b64_tr_b16 v[34:35], v13 offset:43008\n"
    "v_mfma_f32_16x16x32_bf16 v[20:23], v[40:43], v[96:99], v[20:23]\n"
    "ds_read_b64_tr_b16 v[36:37], v13 offset:45056\n"
    "v_mfma_f32_16x16x32_bf16 v[20:23], v[44:47], v[104:107], v[20:23]\n"
    "ds_read_b64_tr_b16 v[38:39], v13 offset:47104\n"
    "s_waitcnt lgkmcnt(8)\n"
    "v_mfma_f32_16x16x32_bf16 v[20:23], v[48:51], v[112:115], v[20:23]\n"
    "ds_read_b64_tr_b16 v[40:41], v13 offset:49152\n"
    "v_mfma_f32_16x16x32_bf16 v[20:23], v[52:55], v[120:123], v[20:23]\n"
    "ds_read_b64_tr_b16 v[42:43], v13 offset:51200\n"
    "v_mfma_f32_16x16x32_bf16 v[28:31], v[48:51], v[116:119], v[28:31]\n"
    "ds_read_b64_tr_b16 v[44:45], v13 offset:53248\n"
    "v_mfma_f32_16x16x32_bf16 v[28:31], v[52:55], v[124:127], v[28:31]\n"
    "ds_read_b64_tr_b16 v[46:47], v13 offset:55296\n"
    "s_waitcnt lgkmcnt(8)\n"
    "v_mfma_f32_16x16x32_bf16 v[28:31], v[56:59], v[132:135], v[28:31]\n"
    "v_add_u32 v14, 4294864896, v14\n"
    "ds_read_b64_tr_b16 v[48:49], v14 offset:0\n"
    "v_mfma_f32_16x16x32_bf16 v[28:31], v[60:63], v[140:143], v[28:31]\n"
    "ds_read_b64_tr_b16 v[50:51], v14 offset:2048\n"
    "v_mfma_f32_16x16x32_bf16 v[20:23], v[56:59], v[128:131], v[20:23]\n"
    "ds_read_b64_tr_b16 v[52:53], v14 offset:4096\n"
    "v_mfma_f32_16x16x32_bf16 v[20:23], v[60:63], v[136:139], v[20:23]\n"
    "ds_read_b64_tr_b16 v[54:55], v14 offset:6144\n"
    "s_waitcnt lgkmcnt(8)\n"
    "v_mfma_f32_16x16x32_bf16 v[20:23], v[32:35], v[144:147], v[20:23]\n"
    "v_add_u32 v15, 4294864896, v15\n"
    "ds_read_b64_tr_b16 v[56:57], v15 offset:0\n"
    "v_mfma_f32_16x16x32_bf16 v[20:23], v[36:39], v[152:155], v[20:23]\n"
    "ds_read_b64_tr_b16 v[58:59], v15 offset:2048\n"
    "v_mfma_f32_16x16x32_bf16 v[28:31], v[32:35], v[148:151], v[28:31]\n"
    "ds_read_b64_tr_b16 v[60:61], v15 offset:4096\n"
    "v_mfma_f32_16x16x32_bf16 v[28:31], v[36:39], v[156:159], v[28:31]\n"
    "ds_read_b64_tr_b16 v[62:63], v15 offset:6144\n"
    "s_waitcnt lgkmcnt(8)\n"
    "v_mfma_f32_16x16x32_bf16 v[28:31], v[40:43], v[164:167], v[28:31]\n"
    "ds_read_b64_tr_b16 v[32:33], v14 offset:8192\n"
    "v_mfma_f32_16x16x32_bf16 v[28:31], v[44:47], v[172:175], v[28:31]\n"
    "ds_read_b64_tr_b16 v[34:35], v14 offset:10240\n"
    "v_mfma_f32_16x16x32_bf16 v[20:23], v[40:43], v[160:163], v[20:23]\n"
    "ds_read_b64_tr_b16 v[36:37], v14 offset:12288\n"
    "v_mfma_f32_16x16x32_bf16 v[20:23], v[44:47], v[168:171], v[20:23]\n"
    "ds_read_b64_tr_b16 v[38:39], v14 offset:14336\n"
    "s_waitcnt lgkmcnt(8)\n"
    "v_mfma_f32_16x16x32_bf16 v[240:243], v[48:51], v[64:67], 0\n"
    "ds_read_b64_tr_b16 v[40:41], v15 offset:8192\n"
    "v_mfma_f32_16x16x32_bf16 v[240:243], v[52:55], v[72:75], v[240:243]\n"
    "ds_read_b64_tr_b16 v[42:43], v15 offset:10240\n"
    "v_mfma_f32_16x16x32_bf16 v[248:251], v[48:51], v[68:71], 0\n"
    "ds_read_b64_tr_b16 v[44:45], v15 offset:12288\n"
    "v_mfma_f32_16x16x32_bf16 v[248:251], v[52:55], v[76:79], v[248:251]\n"
    "ds_read_b64_tr_b16 v[46:47], v15 offset:14336\n"
    "s_waitcnt lgkmcnt(8)\n"
    "v_mfma_f32_16x16x32_bf16 v[252:255], v[56:59], v[68:71], 0\n"
    "ds_read_b64_tr_b16 v[48:49], v14 offset:16384\n"
    "v_mfma_f32_16x16x32_bf16 v[252:255], v[60:63], v[76:79], v[252:255]\n"
    "ds_read_b64_tr_b16 v[50:51], v14 offset:18432\n"
    "buffer_load_dwordx2 v[8:9], v4, s[16:19], s55 offen sc1\n"
    "v_mfma_f32_16x16x32_bf16 v[244:247], v[56:59], v[64:67], 0\n"
    "ds_read_b64_tr_b16 v[52:53], v14 offset:20480\n"
    "v_mfma_f32_16x16x32_bf16 v[244:247], v[60:63], v[72:75], v[244:247]\n"
    "ds_read_b64_tr_b16 v[54:55], v14 offset:22528\n"
    "v_cvt_pk_bf16_f32 v16, v16, v17\n"
    "v_cvt_pk_bf16_f32 v17, v18, v19\n"
    "s_waitcnt lgkmcnt(8)\n"
    "v_mfma_f32_16x16x32_bf16 v[240:243], v[32:35], v[80:83], v[240:243]\n"
    "ds_read_b64_tr_b16 v[56:57], v15 offset:16384\n"
    "v_mfma_f32_16x16x32_bf16 v[240:243], v[36:39], v[88:91], v[240:243]\n"
    "ds_read_b64_tr_b16 v[58:59], v15 offset:18432\n"
    "v_cvt_pk_bf16_f32 v18, v20, v21\n"
    "v_cvt_pk_bf16_f32 v19, v22, v23\n"
    "v_mfma_f32_16x16x32_bf16 v[248:251], v[32:35], v[84:87], v[248:251]\n"
    "ds_read_b64_tr_b16 v[60:61], v15 offset:20480\n"
    "v_mfma_f32_16x16x32_bf16 v[248:251], v[36:39], v[92:95], v[248:251]\n"
    "ds_read_b64_tr_b16 v[62:63], v15 offset:22528\n"
    "v_cvt_pk_bf16_f32 v20, v24, v25\n"
    "v_cvt_pk_bf16_f32 v21, v26, v27\n"
    "s_waitcnt lgkmcnt(8)\n"
    "v_mfma_f32_16x16x32_bf16 v[252:255], v[40:43], v[84:87], v[252:255]\n"
    "ds_read_b64_tr_b16 v[32:33], v14 offset:24576\n"
    "v_mfma_f32_16x16x32_bf16 v[252:255], v[44:47], v[92:95], v[252:255]\n"
    "ds_read_b64_tr_b16 v[34:35], v14 offset:26624\n"
    "v_cvt_pk_bf16_f32 v22, v28, v29\n"
    "v_cvt_pk_bf16_f32 v23, v30, v31\n"
    "v_mfma_f32_16x16x32_bf16 v[244:247], v[40:43], v[80:83], v[244:247]\n"
    "ds_read_b64_tr_b16 v[36:37], v14 offset:28672\n"
    "v_mfma_f32_16x16x32_bf16 v[244:247], v[44:47], v[88:91], v[244:247]\n"
    "ds_read_b64_tr_b16 v[38:39], v14 offset:30720\n"
    "buffer_store_dwordx4 v[16:19], v6, s[16:19], s78 offen offset:0 sc1\n"
    "buffer_store_dwordx4 v[20:23], v6, s[16:19], s78 offen offset:256 sc1\n"
    "s_waitcnt lgkmcnt(8)\n"
    "v_mfma_f32_16x16x32_bf16 v[240:243], v[48:51], v[96:99], v[240:243]\n"
    "ds_read_b64_tr_b16 v[40:41], v15 offset:24576\n"
    "v_mfma_f32_16x16x32_bf16 v[240:243], v[52:55], v[104:107], v[240:243]\n"
    "ds_read_b64_tr_b16 v[42:43], v15 offset:26624\n"
    "v_mfma_f32_16x16x32_bf16 v[248:251], v[48:51], v[100:103], v[248:251]\n"
    "ds_read_b64_tr_b16 v[44:45], v15 offset:28672\n"
    "v_mfma_f32_16x16x32_bf16 v[248:251], v[52:55], v[108:111], v[248:251]\n"
    "ds_read_b64_tr_b16 v[46:47], v15 offset:30720\n"
    "s_waitcnt lgkmcnt(8)\n"
    "v_mfma_f32_16x16x32_bf16 v[252:255], v[56:59], v[100:103], v[252:255]\n"
    "ds_read_b64_tr_b16 v[48:49], v14 offset:32768\n"
    "v_mfma_f32_16x16x32_bf16 v[252:255], v[60:63], v[108:111], v[252:255]\n"
    "ds_read_b64_tr_b16 v[50:51], v14 offset:34816\n"
    "v_mfma_f32_16x16x32_bf16 v[244:247], v[56:59], v[96:99], v[244:247]\n"
    "ds_read_b64_tr_b16 v[52:53], v14 offset:36864\n"
    "v_mfma_f32_16x16x32_bf16 v[244:247], v[60:63], v[104:107], v[244:247]\n"
    "ds_read_b64_tr_b16 v[54:55], v14 offset:38912\n"
    "s_waitcnt lgkmcnt(8)\n"
    "v_mfma_f32_16x16x32_bf16 v[240:243], v[32:35], v[112:115], v[240:243]\n"
    "ds_read_b64_tr_b16 v[56:57], v15 offset:32768\n"
    "v_mfma_f32_16x16x32_bf16 v[240:243], v[36:39], v[120:123], v[240:243]\n"
    "ds_read_b64_tr_b16 v[58:59], v15 offset:34816\n"
    "v_mfma_f32_16x16x32_bf16 v[248:251], v[32:35], v[116:119], v[248:251]\n"
    "ds_read_b64_tr_b16 v[60:61], v15 offset:36864\n"
    "v_mfma_f32_16x16x32_bf16 v[248:251], v[36:39], v[124:127], v[248:251]\n"
    "ds_read_b64_tr_b16 v[62:63], v15 offset:38912\n"
    "s_waitcnt lgkmcnt(8)\n"
    "v_mfma_f32_16x16x32_bf16 v[252:255], v[40:43], v[116:119], v[252:255]\n"
    "ds_read_b64_tr_b16 v[32:33], v14 offset:40960\n"
    "v_mfma_f32_16x16x32_bf16 v[252:255], v[44:47], v[124:127], v[252:255]\n"
    "ds_read_b64_tr_b16 v[34:35], v14 offset:43008\n"
    "v_mfma_f32_16x16x32_bf16 v[244:247], v[40:43], v[112:115], v[244:247]\n"
    "ds_read_b64_tr_b16 v[36:37], v14 offset:45056\n"
    "v_mfma_f32_16x16x32_bf16 v[244:247], v[44:47], v[120:123], v[244:247]\n"
    "ds_read_b64_tr_b16 v[38:39], v14 offset:47104\n"
    "s_waitcnt lgkmcnt(8)\n"
    "v_mfma_f32_16x16x32_bf16 v[240:243], v[48:51], v[128:131], v[240:243]\n"
    "ds_read_b64_tr_b16 v[40:41], v15 offset:40960\n"
    "v_mfma_f32_16x16x32_bf16 v[240:243], v[52:55], v[136:139], v[240:243]\n"
    "ds_read_b64_tr_b16 v[42:43], v15 offset:43008\n"
    "s_waitcnt vmcnt(2)\n"
    "v_cmp_ne_u64 vcc, s[48:49], v[8:9]\n"
    "s_cmp_eq_u32 s68, 3\n"
    "s_cselect_b64 vcc, -1, vcc\n"
    "s_cmp_lg_u64 vcc, 0\n"
    "s_cselect_b64 s[50:51], 0, -1\n"
    "v_mfma_f32_16x16x32_bf16 v[248:251], v[48:51], v[132:135], v[248:251]\n"
    "ds_read_b64_tr_b16 v[44:45], v15 offset:45056\n"
    "v_mfma_f32_16x16x32_bf16 v[248:251], v[52:55], v[140:143], v[248:251]\n"
    "ds_read_b64_tr_b16 v[46:47], v15 offset:47104\n"
    "s_mov_b64 exec, s[50:51]\n"
    "buffer_load_dwordx4 v[64:67], v10, s[16:19], s77 offen offset:0 sc1\n"
    "buffer_load_dwordx4 v[68:71], v10, s[16:19], s77 offen offset:1024 sc1\n"
    "buffer_load_dwordx4 v[72:75], v10, s[16:19], s77 offen offset:2048 sc1\n"
    "buffer_load_dwordx4 v[76:79], v10, s[16:19], s77 offen offset:3072 sc1\n"
    "s_mov_b64 exec, -1\n"
    "s_waitcnt lgkmcnt(8)\n"
    "v_mfma_f32_16x16x32_bf16 v[252:255], v[56:59], v[132:135], v[252:255]\n"
    "ds_read_b64_tr_b16 v[48:49], v14 offset:49152\n"
    "v_mfma_f32_16x16x32_bf16 v[252:255], v[60:63], v[140:143], v[252:255]\n"
    "ds_read_b64_tr_b16 v[50:51], v14 offset:51200\n"
    "v_mfma_f32_16x16x32_bf16 v[244:247], v[56:59], v[128:131], v[244:247]\n"
    "ds_read_b64_tr_b16 v[52:53], v14 offset:53248\n"
    "v_mfma_f32_16x16x32_bf16 v[244:247], v[60:63], v[136:139], v[244:247]\n"
    "ds_read_b64_tr_b16 v[54:55], v14 offset:55296\n"
    "s_mov_b64 exec, s[50:51]\n"
    "buffer_load_dwordx4 v[80:83], v10, s[16:19], s76 offen offset:0 sc1\n"
    "buffer_load_dwordx4 v[84:87], v10, s[16:19], s76 offen offset:1024 sc1\n"
    "buffer_load_dwordx4 v[88:91], v10, s[16:19], s76 offen offset:2048 sc1\n"
    "buffer_load_dwordx4 v[92:95], v10, s[16:19], s76 offen offset:3072 sc1\n"
    "s_mov_b64 exec, -1\n"
    "s_waitcnt lgkmcnt(8)\n"
    "v_mfma_f32_16x16x32_bf16 v[240:243], v[32:35], v[144:147], v[240:243]\n"
    "ds_read_b64_tr_b16 v[56:57], v15 offset:49152\n"
    "v_mfma_f32_16x16x32_bf16 v[240:243], v[36:39], v[152:155], v[240:243]\n"
    "ds_read_b64_tr_b16 v[58:59], v15 offset:51200\n"
    "v_mfma_f32_16x16x32_bf16 v[248:251], v[32:35], v[148:151], v[248:251]\n"
    "ds_read_b64_tr_b16 v[60:61], v15 offset:53248\n"
    "v_mfma_f32_16x16x32_bf16 v[248:251], v[36:39], v[156:159], v[248:251]\n"
    "ds_read_b64_tr_b16 v[62:63], v15 offset:55296\n"
    "s_mov_b64 exec, s[50:51]\n"
    "buffer_load_dwordx4 v[96:99], v11, s[16:19], s77 offen offset:0 sc1\n"
    "buffer_load_dwordx4 v[100:103], v11, s[16:19], s77 offen offset:1024 sc1\n"
    "buffer_load_dwordx4 v[104:107], v11, s[16:19], s77 offen offset:2048 sc1\n"
    "buffer_load_dwordx4 v[108:111], v11, s[16:19], s77 offen offset:3072 sc1\n"
    "s_mov_b64 exec, -1\n"
    "s_waitcnt lgkmcnt(8)\n"
    "v_mfma_f32_16x16x32_bf16 v[252:255], v[40:43], v[148:151], v[252:255]\n"
    "v_mfma_f32_16x16x32_bf16 v[252:255], v[44:47], v[156:159], v[252:255]\n"
    "v_mfma_f32_16x16x32_bf16 v[244:247], v[40:43], v[144:147], v[244:247]\n"
    "v_mfma_f32_16x16x32_bf16 v[244:247], v[44:47], v[152:155], v[244:247]\n"
    "s_mov_b64 exec, s[50:51]\n"
    "buffer_load_dwordx4 v[112:115], v11, s[16:19], s76 offen offset:0 sc1\n"
    "buffer_load_dwordx4 v[116:119], v11, s[16:19], s76 offen offset:1024 sc1\n"
    "buffer_load_dwordx4 v[120:123], v11, s[16:19], s76 offen offset:2048 sc1\n"
    "buffer_load_dwordx4 v[124:127], v11, s[16:19], s76 offen offset:3072 sc1\n"
    "s_mov_b64 exec, -1\n"
    "s_waitcnt lgkmcnt(4)\n"
    "v_mfma_f32_16x16x32_bf16 v[240:243], v[48:51], v[160:163], v[240:243]\n"
    "v_mfma_f32_16x16x32_bf16 v[240:243], v[52:55], v[168:171], v[240:243]\n"
    "v_mfma_f32_16x16x32_bf16 v[248:251], v[48:51], v[164:167], v[248:251]\n"
    "v_mfma_f32_16x16x32_bf16 v[248:251], v[52:55], v[172:175], v[248:251]\n"
    "s_waitcnt lgkmcnt(0)\n"
    "v_mfma_f32_16x16x32_bf16 v[252:255], v[56:59], v[164:167], v[252:255]\n"
    "v_mfma_f32_16x16x32_bf16 v[252:255], v[60:63], v[172:175], v[252:255]\n"
    "v_mfma_f32_16x16x32_bf16 v[244:247], v[56:59], v[160:163], v[244:247]\n"
    "v_mfma_f32_16x16x32_bf16 v[244:247], v[60:63], v[168:171], v[244:247]\n"
    "s_nop 0\n"
    "v_cvt_pk_bf16_f32 v240, v240, v241\n"
    "v_cvt_pk_bf16_f32 v241, v242, v243\n"
    "s_nop 4\n"
    "v_cvt_pk_bf16_f32 v242, v244, v245\n"
    "v_cvt_pk_bf16_f32 v243, v246, v247\n"
    "v_cvt_pk_bf16_f32 v244, v248, v249\n"
    "v_cvt_pk_bf16_f32 v245, v250, v251\n"
    "v_cvt_pk_bf16_f32 v246, v252, v253\n"
    "v_cvt_pk_bf16_f32 v247, v254, v255\n"
    "s_add_u32 s53, s78, 8192\n"
    "buffer_store_dwordx4 v[240:243], v6, s[16:19], s53 offen offset:0 sc1\n"
    "buffer_store_dwordx4 v[244:247], v6, s[16:19], s53 offen offset:256 sc1\n"
    "s_cmp_eq_u32 s68, 3\n"
    "s_cbranch_scc0 .Lf_out\n"
    "s_waitcnt vmcnt(0)\n"
    "v_mov_b64 v[8:9], s[48:49]\n"
    "s_mov_b64 exec, 1\n"
    "buffer_store_dwordx2 v[8:9], off, s[16:19], s40 sc1\n"
    "s_mov_b64 exec, -1\n"
    "s_branch .Lf_end\n"
    ".Lf_out:\n"
    "s_cmp_lg_u64 s[50:51], 0\n"
    "s_cbranch_scc1 .Lf_ook\n"
    "s_waitcnt vmcnt(0)\n"
    "v_mov_b64 v[8:9], s[48:49]\n"
    "s_mov_b64 exec, 1\n"
    "buffer_store_dwordx2 v[8:9], off, s[16:19], s40 sc1\n"
    "s_mov_b64 exec, -1\n"
    "s_mov_b32 s40, 0x7f000000\n"
    "s_mov_b32 s63, 0x20000\n"
    ".Lpf_38:\n"
    "buffer_load_dwordx2 v[8:9], v4, s[16:19], s55 offen sc1\n"
    "s_waitcnt vmcnt(0)\n"
    "v_cmp_ne_u64 vcc, s[48:49], v[8:9]\n"
    "s_cbranch_vccz .Lpfd_39\n"
    "s_sleep 1\n"
    "s_sub_u32 s63, s63, 1\n"
    "s_cbranch_scc0 .Lpf_38\n"
    ".Lpfd_39:\n"
    "buffer_load_dwordx4 v[64:67], v10, s[16:19], s77 offen offset:0 sc1\n"
    "buffer_load_dwordx4 v[68:71], v10, s[16:19], s77 offen offset:1024 sc1\n"
    "buffer_load_dwordx4 v[72:75], v10, s[16:19], s77 offen offset:2048 sc1\n"
    "buffer_load_dwordx4 v[76:79], v10, s[16:19], s77 offen offset:3072 sc1\n"
    "buffer_load_dwordx4 v[80:83], v10, s[16:19], s76 offen offset:0 sc1\n"
    "buffer_load_dwordx4 v[84:87], v10, s[16:19], s76 offen offset:1024 sc1\n"
    "buffer_load_dwordx4 v[88:91], v10, s[16:19], s76 offen offset:2048 sc1\n"
    "buffer_load_dwordx4 v[92:95], v10, s[16:19], s76 offen offset:3072 sc1\n"
    "buffer_load_dwordx4 v[96:99], v11, s[16:19], s77 offen offset:0 sc1\n"
    "buffer_load_dwordx4 v[100:103], v11, s[16:19], s77 offen offset:1024 sc1\n"
    "buffer_load_dwordx4 v[104:107], v11, s[16:19], s77 offen offset:2048 sc1\n"
    "buffer_load_dwordx4 v[108:111], v11, s[16:19], s77 offen offset:3072 sc1\n"
    "buffer_load_dwordx4 v[112:115], v11, s[16:19], s76 offen offset:0 sc1\n"
    "buffer_load_dwordx4 v[116:119], v11, s[16:19], s76 offen offset:1024 sc1\n"
    "buffer_load_dwordx4 v[120:123], v11, s[16:19], s76 offen offset:2048 sc1\n"
    "buffer_load_dwordx4 v[124:127], v11, s[16:19], s76 offen offset:3072 sc1\n"
    "buffer_store_dword v8, off, s[16:19], s40\n"
    "buffer_store_dword v8, off, s[16:19], s40\n"
    ".Lf_ook:\n"
    "s_waitcnt vmcnt(17)\n"
    "v_mfma_f32_32x32x16_bf16 v[32:47], v[176:179], v[64:67], 0\n"
    "v_mov_b32 v20, 163328\n"
    "ds_read_b128 v[16:19], v20\n"
    "s_mul_i32 s53, s47, 12288\n"
    "s_add_u32 s53, s53, 106496\n"
    "v_add_u32 v6, s53, v2\n"
    "v_mfma_f32_32x32x16_bf16 v[48:63], v[180:183], v[64:67], 0\n"
    "s_lshr_b32 s52, s47, 1\n"
    "s_lshl1_add_u32 s52, s72, s52\n"
    "s_and_b32 s53, s47, 1\n"
    "s_lshl1_add_u32 s53, s73, s53\n"
    "s_mul_i32 s78, s52, 14\n"
    "s_waitcnt vmcnt(16)\n"
    "v_mfma_f32_32x32x16_bf16 v[32:47], v[184:187], v[68:71], v[32:47]\n"
    "s_add_u32 s78, s78, s53\n"
    "s_lshl_b32 s78, s78, 13\n"
    "s_add_u32 s78, s78, 16777216\n"
    "s_add_u32 s75, s78, 4096\n"
    "s_lshr_b32 s54, s73, 1\n"
    "v_mfma_f32_32x32x16_bf16 v[48:63], v[188:191], v[68:71], v[48:63]\n"
    "s_lshl2_add_u32 s54, s52, s54\n"
    "s_lshl_b32 s54, s54, 7\n"
    "s_add_u32 s54, s54, 507904\n"
    "s_lshl_b32 s56, s52, 7\n"
    "s_add_u32 s56, s56, 4096\n"
    "s_waitcnt vmcnt(15)\n"
    "v_mfma_f32_32x32x16_bf16 v[32:47], v[192:195], v[72:75], v[32:47]\n"
    "s_mul_i32 s52, s52, 28672\n"
    "s_lshl_b32 s53, s53, 6\n"
    "s_add_u32 s52, s52, s53\n"
    "v_mul_u32_u24 v4, 896, v7\n"
    "v_lshl_add_u32 v4, v3, 4, v4\n"
    "v_mfma_f32_32x32x16_bf16 v[48:63], v[196:199], v[72:75], v[48:63]\n"
    "v_lshlrev_b32 v14, 2, v7\n"
    "v_bfe_u32 v10, v7, 3, 1\n"
    "v_cmp_eq_u32 vcc, v10, v3\n"
    "v_and_b32 v10, 1, v7\n"
    "v_lshlrev_b32 v10, 4, v10\n"
    "s_waitcnt vmcnt(14)\n"
    "v_mfma_f32_32x32x16_bf16 v[32:47], v[200:203], v[76:79], v[32:47]\n"
    "v_mov_b32 v11, 0x3f80\n"
    "v_lshlrev_b32 v10, v10, v11\n"
    "v_cndmask_b32 v10, 0, v10, vcc\n"
    "v_lshrrev_b32 v11, 4, v7\n"
    "v_bfe_u32 v12, v7, 1, 1\n"
    "v_mfma_f32_32x32x16_bf16 v[48:63], v[204:207], v[76:79], v[48:63]\n"
    "v_lshl_add_u32 v11, v11, 1, v12\n"
    "v_and_or_b32 v11, v7, 4, v11\n"
    "v_cmp_eq_u32 vcc, 0, v11\n"
    "v_cmp_eq_u32 s[50:51], 1, v11\n"
    "s_nop 0\n"
    "s_waitcnt vmcnt(13)\n"
    "v_mfma_f32_32x32x16_bf16 v[32:47], v[208:211], v[80:83], v[32:47]\n"
    "v_cndmask_b32 v248, 0, v10, vcc\n"
    "v_cndmask_b32 v249, 0, v10, s[50:51]\n"
    "v_cmp_eq_u32 vcc, 2, v11\n"
    "v_cmp_eq_u32 s[50:51], 3, v11\n"
    "s_nop 0\n"
    "v_mfma_f32_32x32x16_bf16 v[48:63], v[212:215], v[80:83], v[48:63]\n"
    "v_cndmask_b32 v250, 0, v10, vcc\n"
    "v_cndmask_b32 v251, 0, v10, s[50:51]\n"
    "v_cmp_eq_u32 vcc, 4, v11\n"
    "v_cmp_eq_u32 s[50:51], 5, v11\n"
    "s_nop 0\n"
    "s_waitcnt vmcnt(12)\n"
    "v_mfma_f32_32x32x16_bf16 v[32:47], v[216:219], v[84:87], v[32:47]\n"
    "v_cndmask_b32 v252, 0, v10, vcc\n"
    "v_cndmask_b32 v253, 0, v10, s[50:51]\n"
    "v_cmp_eq_u32 vcc, 6, v11\n"
    "v_cmp_eq_u32 s[50:51], 7, v11\n"
    "s_nop 0\n"
    "v_mfma_f32_32x32x16_bf16 v[48:63], v[220:223], v[84:87], v[48:63]\n"
    "v_cndmask_b32 v254, 0, v10, vcc\n"
    "v_cndmask_b32 v255, 0, v10, s[50:51]\n"
    "s_xor_b32 s53, s47, 3\n"
    "s_mul_i32 s53, s53, 12288\n"
    "s_add_u32 s53, s53, 106496\n"
    "s_waitcnt vmcnt(11)\n"
    "v_mfma_f32_32x32x16_bf16 v[32:47], v[224:227], v[88:91], v[32:47]\n"
    "v_add_u32 v11, s53, v2\n"
    "s_xor_b32 s53, s47, 2\n"
    "s_mul_i32 s53, s53, 12288\n"
    "s_add_u32 s53, s53, 106496\n"
    "v_add_u32 v12, s53, v2\n"
    "v_mfma_f32_32x32x16_bf16 v[48:63], v[228:231], v[88:91], v[48:63]\n"
    "s_xor_b32 s53, s47, 1\n"
    "s_mul_i32 s53, s53, 12288\n"
    "s_add_u32 s53, s53, 106496\n"
    "v_add_u32 v13, s53, v2\n"
    "s_waitcnt vmcnt(10)\n"
    "v_mfma_f32_32x32x16_bf16 v[32:47], v[232:235], v[92:95], v[32:47]\n"
    "v_mfma_f32_32x32x16_bf16 v[48:63], v[236:239], v[92:95], v[48:63]\n"
    "s_waitcnt lgkmcnt(0)\n"
    "v_min3_u32 v16, v16, v17, v18\n"
    "v_min_u32 v16, v16, v19\n"
    "v_cmp_gt_u32 vcc, 9, v16\n"
    "s_cbranch_vccz .Lf_lrok\n"
    "v_mov_b32 v20, 163328\n"
    "s_mov_b32 s63, 0x100000\n"
    ".Lwp_40:\n"
    "ds_read_b128 v[16:19], v20\n"
    "s_waitcnt lgkmcnt(0)\n"
    "v_min3_u32 v21, v16, v17, v18\n"
    "v_min_u32 v21, v21, v19\n"
    "v_cmp_gt_u32 vcc, 9, v21\n"
    "s_cbranch_vccz .Lwpd_41\n"
    "s_sub_u32 s63, s63, 1\n"
    "s_cbranch_scc0 .Lwp_40\n"
    ".Lwpd_41:\n"
    ".Lf_lrok:\n"
    "s_waitcnt vmcnt(9)\n"
    "s_waitcnt vmcnt(0)\n"
    "v_mov_b64 v[8:9], s[48:49]\n"
    "s_mov_b64 exec, 1\n"
    "buffer_store_dwordx2 v[8:9], off, s[16:19], s40 sc1\n"
    "s_mov_b64 exec, -1\n"
    "v_mfma_f32_32x32x16_bf16 v[144:159], v[176:179], v[96:99], 0\n"
    "s_nop 5\n"
    "v_mfma_f32_32x32x16_bf16 v[144:159], v[184:187], v[100:103], v[144:159]\n"
    "s_nop 5\n"
    "v_mfma_f32_32x32x16_bf16 v[144:159], v[192:195], v[104:107], v[144:159]\n"
    "ds_write_b128 v6, v[32:35] offset:0\n"
    "v_mfma_f32_32x32x16_bf16 v[144:159], v[200:203], v[108:111], v[144:159]\n"
    "ds_write_b128 v6, v[36:39] offset:1024\n"
    "v_mfma_f32_32x32x16_bf16 v[144:159], v[208:211], v[112:115], v[144:159]\n"
    "ds_write_b128 v6, v[40:43] offset:2048\n"
    "v_mfma_f32_32x32x16_bf16 v[144:159], v[216:219], v[116:119], v[144:159]\n"
    "ds_write_b128 v6, v[44:47] offset:3072\n"
    "v_mfma_f32_32x32x16_bf16 v[144:159], v[224:227], v[120:123], v[144:159]\n"
    "ds_write_b128 v6, v[48:51] offset:4096\n"
    "v_mfma_f32_32x32x16_bf16 v[144:159], v[232:235], v[124:127], v[144:159]\n"
    "ds_write_b128 v6, v[52:55] offset:5120\n"
    "v_mfma_f32_32x32x16_bf16 v[160:175], v[180:183], v[96:99], 0\n"
    "s_nop 5\n"
    "v_mfma_f32_32x32x16_bf16 v[160:175], v[188:191], v[100:103], v[160:175]\n"
    "s_nop 5\n"
    "v_mfma_f32_32x32x16_bf16 v[160:175], v[196:199], v[104:107], v[160:175]\n"
    "ds_write_b128 v6, v[56:59] offset:6144\n"
    "buffer_load_dwordx2 v[8:9], v5, s[16:19], s54 offen sc1\n"
    "v_mfma_f32_32x32x16_bf16 v[160:175], v[204:207], v[108:111], v[160:175]\n"
    "ds_write_b128 v6, v[60:63] offset:7168\n"
    "v_mfma_f32_32x32x16_bf16 v[160:175], v[212:215], v[112:115], v[160:175]\n"
    "ds_write_b128 v6, v[144:147] offset:8192\n"
    "v_mfma_f32_32x32x16_bf16 v[160:175], v[220:223], v[116:119], v[160:175]\n"
    "ds_write_b128 v6, v[148:151] offset:9216\n"
    "v_mfma_f32_32x32x16_bf16 v[160:175], v[228:231], v[120:123], v[160:175]\n"
    "ds_write_b128 v6, v[152:155] offset:10240\n"
    "v_mfma_f32_32x32x16_bf16 v[160:175], v[236:239], v[124:127], v[160:175]\n"
    "ds_write_b128 v6, v[156:159] offset:11264\n"
    "s_waitcnt lgkmcnt(0)\n"
    "s_barrier\n"
    "ds_read_b128 v[32:35], v11 offset:0\n"
    "ds_read_b128 v[48:51], v12 offset:4096\n"
    "ds_read_b128 v[144:147], v13 offset:8192\n"
    "ds_read_b128 v[36:39], v11 offset:1024\n"
    "ds_read_b128 v[52:55], v12 offset:5120\n"
    "ds_read_b128 v[148:151], v13 offset:9216\n"
    "ds_read_b128 v[40:43], v11 offset:2048\n"
    "ds_read_b128 v[56:59], v12 offset:6144\n"
    "ds_read_b128 v[152:155], v13 offset:10240\n"
    "ds_read_b128 v[44:47], v11 offset:3072\n"
    "ds_read_b128 v[60:63], v12 offset:7168\n"
    "ds_read_b128 v[156:159], v13 offset:11264\n"
    "s_waitcnt vmcnt(0)\n"
    "v_cmp_ne_u64 vcc, s[48:49], v[8:9]\n"
    "s_cbranch_vccz .Lf_pok\n"
    "s_mov_b32 s63, 0x20000\n"
    ".Lpf_42:\n"
    "buffer_load_dwordx2 v[8:9], v5, s[16:19], s54 offen sc1\n"
    "s_waitcnt vmcnt(0)\n"
    "v_cmp_ne_u64 vcc, s[48:49], v[8:9]\n"
    "s_cbranch_vccz .Lpfd_43\n"
    "s_sub_u32 s63, s63, 1\n"
    "s_cbranch_scc0 .Lpf_42\n"
    ".Lpfd_43:\n"
    ".Lf_pok:\n"
    "buffer_load_dword v14, v14, s[16:19], s56 offen sc1\n"
    "buffer_load_dwordx4 v[128:131], v2, s[16:19], s78 offen offset:0 sc1\n"
    "buffer_load_dwordx4 v[132:135], v2, s[16:19], s78 offen offset:1024 sc1\n"
    "buffer_load_dwordx4 v[136:139], v2, s[16:19], s78 offen offset:2048 sc1\n"
    "buffer_load_dwordx4 v[140:143], v2, s[16:19], s78 offen offset:3072 sc1\n"
    "buffer_load_dwordx4 v[16:19], v2, s[16:19], s75 offen offset:0 sc1\n"
    "buffer_load_dwordx4 v[20:23], v2, s[16:19], s75 offen offset:1024 sc1\n"
    "buffer_load_dwordx4 v[24:27], v2, s[16:19], s75 offen offset:2048 sc1\n"
    "buffer_load_dwordx4 v[28:31], v2, s[16:19], s75 offen offset:3072 sc1\n"
    "s_waitcnt lgkmcnt(11)\n"
    "v_pk_add_f32 v[160:161], v[160:161], v[32:33]\n"
    "v_pk_add_f32 v[162:163], v[162:163], v[34:35]\n"
    "s_waitcnt lgkmcnt(10)\n"
    "v_pk_add_f32 v[160:161], v[160:161], v[48:49]\n"
    "v_pk_add_f32 v[162:163], v[162:163], v[50:51]\n"
    "s_waitcnt lgkmcnt(9)\n"
    "v_pk_add_f32 v[160:161], v[160:161], v[144:145]\n"
    "v_pk_add_f32 v[162:163], v[162:163], v[146:147]\n"
    "s_waitcnt lgkmcnt(8)\n"
    "v_pk_add_f32 v[164:165], v[164:165], v[36:37]\n"
    "v_pk_add_f32 v[166:167], v[166:167], v[38:39]\n"
    "s_waitcnt lgkmcnt(7)\n"
    "v_pk_add_f32 v[164:165], v[164:165], v[52:53]\n"
    "v_pk_add_f32 v[166:167], v[166:167], v[54:55]\n"
    "s_waitcnt lgkmcnt(6)\n"
    "v_pk_add_f32 v[164:165], v[164:165], v[148:149]\n"
    "v_pk_add_f32 v[166:167], v[166:167], v[150:151]\n"
    "s_waitcnt lgkmcnt(5)\n"
    "v_pk_add_f32 v[168:169], v[168:169], v[40:41]\n"
    "v_pk_add_f32 v[170:171], v[170:171], v[42:43]\n"
    "s_waitcnt lgkmcnt(4)\n"
    "v_pk_add_f32 v[168:169], v[168:169], v[56:57]\n"
    "v_pk_add_f32 v[170:171], v[170:171], v[58:59]\n"
    "s_waitcnt lgkmcnt(3)\n"
    "v_pk_add_f32 v[168:169], v[168:169], v[152:153]\n"
    "v_pk_add_f32 v[170:171], v[170:171], v[154:155]\n"
    "s_waitcnt lgkmcnt(2)\n"
    "v_pk_add_f32 v[172:173], v[172:173], v[44:45]\n"
    "v_pk_add_f32 v[174:175], v[174:175], v[46:47]\n"
    "s_waitcnt lgkmcnt(1)\n"
    "v_pk_add_f32 v[172:173], v[172:173], v[60:61]\n"
    "v_pk_add_f32 v[174:175], v[174:175], v[62:63]\n"
    "s_waitcnt lgkmcnt(0)\n"
    "v_pk_add_f32 v[172:173], v[172:173], v[156:157]\n"
    "v_pk_add_f32 v[174:175], v[174:175], v[158:159]\n"
    "s_waitcnt vmcnt(8)\n"
    "v_sub_f32 v15, 1.0, v14\n"
    "s_waitcnt vmcnt(7)\n"
    "v_mfma_f32_32x32x16_bf16 v[32:47], v[248:251], v[128:131], 0\n"
    "v_mul_f32 v160, v15, v160\n"
    "v_mul_f32 v161, v15, v161\n"
    "s_waitcnt vmcnt(6)\n"
    "v_mfma_f32_32x32x16_bf16 v[32:47], v[252:255], v[132:135], v[32:47]\n"
    "v_mul_f32 v162, v15, v162\n"
    "v_mul_f32 v163, v15, v163\n"
    "s_waitcnt vmcnt(5)\n"
    "v_mfma_f32_32x32x16_bf16 v[32:47], v[248:251], v[136:139], v[32:47]\n"
    "v_mul_f32 v164, v15, v164\n"
    "v_mul_f32 v165, v15, v165\n"
    "s_waitcnt vmcnt(4)\n"
    "v_mfma_f32_32x32x16_bf16 v[32:47], v[252:255], v[140:143], v[32:47]\n"
    "v_mul_f32 v166, v15, v166\n"
    "v_mul_f32 v167, v15, v167\n"
    "s_waitcnt vmcnt(3)\n"
    "v_mfma_f32_32x32x16_bf16 v[32:47], v[248:251], v[16:19], v[32:47]\n"
    "v_mul_f32 v168, v15, v168\n"
    "v_mul_f32 v169, v15, v169\n"
    "s_waitcnt vmcnt(2)\n"
    "v_mfma_f32_32x32x16_bf16 v[32:47], v[252:255], v[20:23], v[32:47]\n"
    "v_mul_f32 v170, v15, v170\n"
    "v_mul_f32 v171, v15, v171\n"
    "s_waitcnt vmcnt(1)\n"
    "v_mfma_f32_32x32x16_bf16 v[32:47], v[248:251], v[24:27], v[32:47]\n"
    "v_mul_f32 v172, v15, v172\n"
    "v_mul_f32 v173, v15, v173\n"
    "s_waitcnt vmcnt(0)\n"
    "v_mfma_f32_32x32x16_bf16 v[32:47], v[252:255], v[28:31], v[32:47]\n"
    "v_mul_f32 v174, v15, v174\n"
    "v_mul_f32 v175, v15, v175\n"
    "s_nop 9\n"
    "v_fmac_f32 v160, v14, v32\n"
    "v_fmac_f32 v161, v14, v33\n"
    "v_fmac_f32 v162, v14, v34\n"
    "v_fmac_f32 v163, v14, v35\n"
    "v_cvt_pk_bf16_f32 v64, v160, v161\n"
    "v_cvt_pk_bf16_f32 v65, v162, v163\n"
    "v_fmac_f32 v164, v14, v36\n"
    "v_fmac_f32 v165, v14, v37\n"
    "v_fmac_f32 v166, v14, v38\n"
    "v_fmac_f32 v167, v14, v39\n"
    "v_cvt_pk_bf16_f32 v66, v164, v165\n"
    "v_cvt_pk_bf16_f32 v67, v166, v167\n"
    "buffer_store_dwordx4 v[64:67], v4, s[36:39], s52 offen offset:0\n"
    "v_fmac_f32 v168, v14, v40\n"
    "v_fmac_f32 v169, v14, v41\n"
    "v_fmac_f32 v170, v14, v42\n"
    "v_fmac_f32 v171, v14, v43\n"
    "v_cvt_pk_bf16_f32 v68, v168, v169\n"
    "v_cvt_pk_bf16_f32 v69, v170, v171\n"
    "v_fmac_f32 v172, v14, v44\n"
    "v_fmac_f32 v173, v14, v45\n"
    "v_fmac_f32 v174, v14, v46\n"
    "v_fmac_f32 v175, v14, v47\n"
    "v_cvt_pk_bf16_f32 v70, v172, v173\n"
    "v_cvt_pk_bf16_f32 v71, v174, v175\n"
    "buffer_store_dwordx4 v[68:71], v4, s[36:39], s52 offen offset:32\n"
    "s_or_b32 s52, s2, s47\n"
    "s_cbranch_scc1 .Lf_end\n"
    "v_mov_b64 v[8:9], s[48:49]\n"
    "s_mov_b64 exec, 1\n"
    "buffer_store_dwordx2 v[8:9], off, s[16:19], s52 sc1\n"
    "s_mov_b64 exec, -1\n"
    ".Lf_end:\n"
    "s_endpgm\n"
    ".Lrare:\n"
    ".Lbad:\n"
    "v_mov_b32 v18, 8\n"
    "ds_write_b32 v0, v18\n"
    "s_mov_b32 s3, 1\n"
    "v_mov_b32 v22, 163344\n"
    "s_mov_b32 s63, 0x100000\n"
    ".Lwp_44:\n"
    "ds_read_b128 v[18:21], v22\n"
    "s_waitcnt lgkmcnt(0)\n"
    "v_min3_u32 v23, v18, v19, v20\n"
    "v_min_u32 v23, v23, v21\n"
    "v_cmp_gt_u32 vcc, 7, v23\n"
    "s_cbranch_vccz .Lwpd_45\n"
    "s_sleep 1\n"
    "s_sub_u32 s63, s63, 1\n"
    "s_cbranch_scc0 .Lwp_44\n"
    ".Lwpd_45:\n"
    ".Lfb:\n"
    "s_waitcnt vmcnt(0)\n"
    "v_mov_b32 v28, 0xff800000\n"
    "v_mov_b32 v29, 0xff800000\n"
    "v_mov_b32 v30, 0xff800000\n"
    "v_mov_b32 v31, 0xff800000\n"
    "s_mov_b32 s52, 0\n"
    "s_mov_b32 s42, 983040\n"
    ".Lfb_loop:\n"
    "buffer_load_dwordx4 v[64:67], v4, s[40:43], s52 offen offset:0 sc1\n"
    "buffer_load_dwordx4 v[68:71], v4, s[40:43], s52 offen offset:1024 sc1\n"
    "s_waitcnt vmcnt(0)\n"
    "v_mfma_f32_32x32x16_bf16 v[192:207], v[64:67], v[32:35], 0\n"
    "v_mfma_f32_32x32x16_bf16 v[192:207], v[68:71], v[36:39], v[192:207]\n"
    "v_mfma_f32_32x32x16_bf16 v[208:223], v[64:67], v[40:43], 0\n"
    "v_mfma_f32_32x32x16_bf16 v[208:223], v[68:71], v[44:47], v[208:223]\n"
    "v_mfma_f32_32x32x16_bf16 v[224:239], v[64:67], v[48:51], 0\n"
    "v_mfma_f32_32x32x16_bf16 v[224:239], v[68:71], v[52:55], v[224:239]\n"
    "v_mfma_f32_32x32x16_bf16 v[240:255], v[64:67], v[56:59], 0\n"
    "v_mfma_f32_32x32x16_bf16 v[240:255], v[68:71], v[60:63], v[240:255]\n"
    "s_nop 5\n"
    "v_max3_f32 v28, v28, v192, v193\n"
    "v_max3_f32 v28, v28, v194, v195\n"
    "v_max3_f32 v28, v28, v196, v197\n"
    "v_max3_f32 v28, v28, v198, v199\n"
    "v_max3_f32 v28, v28, v200, v201\n"
    "v_max3_f32 v28, v28, v202, v203\n"
    "v_max3_f32 v28, v28, v204, v205\n"
    "v_max3_f32 v28, v28, v206, v207\n"
    "v_max3_f32 v29, v29, v208, v209\n"
    "v_max3_f32 v29, v29, v210, v211\n"
    "v_max3_f32 v29, v29, v212, v213\n"
    "v_max3_f32 v29, v29, v214, v215\n"
    "v_max3_f32 v29, v29, v216, v217\n"
    "v_max3_f32 v29, v29, v218, v219\n"
    "v_max3_f32 v29, v29, v220, v221\n"
    "v_max3_f32 v29, v29, v222, v223\n"
    "v_max3_f32 v30, v30, v224, v225\n"
    "v_max3_f32 v30, v30, v226, v227\n"
    "v_max3_f32 v30, v30, v228, v229\n"
    "v_max3_f32 v30, v30, v230, v231\n"
    "v_max3_f32 v30, v30, v232, v233\n"
    "v_max3_f32 v30, v30, v234, v235\n"
    "v_max3_f32 v30, v30, v236, v237\n"
    "v_max3_f32 v30, v30, v238, v239\n"
    "v_max3_f32 v31, v31, v240, v241\n"
    "v_max3_f32 v31, v31, v242, v243\n"
    "v_max3_f32 v31, v31, v244, v245\n"
    "v_max3_f32 v31, v31, v246, v247\n"
    "v_max3_f32 v31, v31, v248, v249\n"
    "v_max3_f32 v31, v31, v250, v251\n"
    "v_max3_f32 v31, v31, v252, v253\n"
    "v_max3_f32 v31, v31, v254, v255\n"
    "s_add_u32 s52, s52, 61440\n"
    "s_cmp_lt_u32 s52, 983040\n"
    "s_cbranch_scc1 .Lfb_loop\n"
    "s_mul_i32 s52, s47, 640\n"
    "v_lshl_add_u32 v12, v7, 2, s52\n"
    "v_mov_b32 v13, v28\n"
    "s_nop 1\n"
    "v_permlane32_swap_b32 v13, v28\n"
    "v_max_f32 v28, v13, v28\n"
    "v_xor_b32 v13, 1536, v12\n"
    "v_add_u32 v13, 159744, v13\n"
    "ds_write_b32 v13, v28\n"
    "v_mov_b32 v13, v29\n"
    "s_nop 1\n"
    "v_permlane32_swap_b32 v13, v29\n"
    "v_max_f32 v29, v13, v29\n"
    "v_xor_b32 v13, 1024, v12\n"
    "v_add_u32 v13, 159744, v13\n"
    "ds_write_b32 v13, v29\n"
    "v_mov_b32 v13, v30\n"
    "s_nop 1\n"
    "v_permlane32_swap_b32 v13, v30\n"
    "v_max_f32 v30, v13, v30\n"
    "v_xor_b32 v13, 512, v12\n"
    "v_add_u32 v13, 159744, v13\n"
    "ds_write_b32 v13, v30\n"
    "v_mov_b32 v13, v31\n"
    "s_nop 1\n"
    "v_permlane32_swap_b32 v13, v31\n"
    "v_max_f32 v31, v13, v31\n"
    "v_xor_b32 v13, 0, v12\n"
    "v_add_u32 v13, 159744, v13\n"
    "ds_write_b32 v13, v31\n"
    "v_mov_b32 v18, 9\n"
    "ds_write_b32 v0, v18\n"
    "v_mov_b32 v22, 163344\n"
    "s_mov_b32 s63, 0x100000\n"
    ".Lwp_46:\n"
    "ds_read_b128 v[18:21], v22\n"
    "s_waitcnt lgkmcnt(0)\n"
    "v_min3_u32 v23, v18, v19, v20\n"
    "v_min_u32 v23, v23, v21\n"
    "v_cmp_gt_u32 vcc, 9, v23\n"
    "s_cbranch_vccz .Lwpd_47\n"
    "s_sleep 1\n"
    "s_sub_u32 s63, s63, 1\n"
    "s_cbranch_scc0 .Lwp_46\n"
    ".Lwpd_47:\n"
    "s_lshl_b32 s52, s47, 9\n"
    "v_lshl_add_u32 v12, v7, 2, s52\n"
    "v_xor_b32 v13, 1536, v12\n"
    "v_add_u32 v13, 159744, v13\n"
    "ds_read_b32 v128, v13 offset:0\n"
    "ds_read_b32 v129, v13 offset:128\n"
    "ds_read_b32 v130, v13 offset:256\n"
    "ds_read_b32 v131, v13 offset:384\n"
    "s_waitcnt lgkmcnt(0)\n"
    "v_max3_f32 v28, v128, v129, v130\n"
    "v_max_f32 v28, v28, v131\n"
    "v_xor_b32 v13, 1024, v12\n"
    "v_add_u32 v13, 159744, v13\n"
    "ds_read_b32 v128, v13 offset:0\n"
    "ds_read_b32 v129, v13 offset:128\n"
    "ds_read_b32 v130, v13 offset:256\n"
    "ds_read_b32 v131, v13 offset:384\n"
    "s_waitcnt lgkmcnt(0)\n"
    "v_max3_f32 v29, v128, v129, v130\n"
    "v_max_f32 v29, v29, v131\n"
    "v_xor_b32 v13, 512, v12\n"
    "v_add_u32 v13, 159744, v13\n"
    "ds_read_b32 v128, v13 offset:0\n"
    "ds_read_b32 v129, v13 offset:128\n"
    "ds_read_b32 v130, v13 offset:256\n"
    "ds_read_b32 v131, v13 offset:384\n"
    "s_waitcnt lgkmcnt(0)\n"
    "v_max3_f32 v30, v128, v129, v130\n"
    "v_max_f32 v30, v30, v131\n"
    "v_xor_b32 v13, 0, v12\n"
    "v_add_u32 v13, 159744, v13\n"
    "ds_read_b32 v128, v13 offset:0\n"
    "ds_read_b32 v129, v13 offset:128\n"
    "ds_read_b32 v130, v13 offset:256\n"
    "ds_read_b32 v131, v13 offset:384\n"
    "s_waitcnt lgkmcnt(0)\n"
    "v_max3_f32 v31, v128, v129, v130\n"
    "v_max_f32 v31, v31, v131\n"
    "v_mov_b64 v[192:193], 0\n"
    "v_mov_b64 v[194:195], 0\n"
    "v_mov_b64 v[196:197], 0\n"
    "v_mov_b64 v[198:199], 0\n"
    "v_mov_b64 v[200:201], 0\n"
    "v_mov_b64 v[202:203], 0\n"
    "v_mov_b64 v[204:205], 0\n"
    "v_mov_b64 v[206:207], 0\n"
    "v_mov_b64 v[208:209], 0\n"
    "v_mov_b64 v[210:211], 0\n"
    "v_mov_b64 v[212:213], 0\n"
    "v_mov_b64 v[214:215], 0\n"
    "v_mov_b64 v[216:217], 0\n"
    "v_mov_b64 v[218:219], 0\n"
    "v_mov_b64 v[220:221], 0\n"
    "v_mov_b64 v[222:223], 0\n"
    "v_mov_b64 v[224:225], 0\n"
    "v_mov_b64 v[226:227], 0\n"
    "v_mov_b64 v[228:229], 0\n"
    "v_mov_b64 v[230:231], 0\n"
    "v_mov_b64 v[232:233], 0\n"
    "v_mov_b64 v[234:235], 0\n"
    "v_mov_b64 v[236:237], 0\n"
    "v_mov_b64 v[238:239], 0\n"
    "v_mov_b64 v[240:241], 0\n"
    "v_mov_b64 v[242:243], 0\n"
    "v_mov_b64 v[244:245], 0\n"
    "v_mov_b64 v[246:247], 0\n"
    "v_mov_b64 v[248:249], 0\n"
    "v_mov_b64 v[250:251], 0\n"
    "v_mov_b64 v[252:253], 0\n"
    "v_mov_b64 v[254:255], 0\n"
    "s_mov_b32 s52, 0\n"
    ".Lfb_loop2:\n"
    "buffer_load_dwordx4 v[64:67], v4, s[40:43], s52 offen offset:0 sc1\n"
    "buffer_load_dwordx4 v[68:71], v4, s[40:43], s52 offen offset:1024 sc1\n"
    "buffer_load_dwordx4 v[72:75], v5, s[40:43], s52 offen offset:2048 sc1\n"
    "buffer_load_dwordx4 v[76:79], v5, s[40:43], s52 offen offset:3072 sc1\n"
    "s_waitcnt vmcnt(0)\n"
    "v_mfma_f32_32x32x16_bf16 v[128:143], v[64:67], v[32:35], 0\n"
    "v_mfma_f32_32x32x16_bf16 v[128:143], v[68:71], v[36:39], v[128:143]\n"
    "v_mfma_f32_32x32x16_bf16 v[144:159], v[64:67], v[40:43], 0\n"
    "v_mfma_f32_32x32x16_bf16 v[144:159], v[68:71], v[44:47], v[144:159]\n"
    "v_mfma_f32_32x32x16_bf16 v[160:175], v[64:67], v[48:51], 0\n"
    "v_mfma_f32_32x32x16_bf16 v[160:175], v[68:71], v[52:55], v[160:175]\n"
    "v_mfma_f32_32x32x16_bf16 v[176:191], v[64:67], v[56:59], 0\n"
    "v_mfma_f32_32x32x16_bf16 v[176:191], v[68:71], v[60:63], v[176:191]\n"
    "s_nop 5\n"
    "v_sub_f32 v128, v128, v28\n"
    "v_sub_f32 v129, v129, v28\n"
    "v_sub_f32 v130, v130, v28\n"
    "v_sub_f32 v131, v131, v28\n"
    "v_sub_f32 v132, v132, v28\n"
    "v_sub_f32 v133, v133, v28\n"
    "v_sub_f32 v134, v134, v28\n"
    "v_sub_f32 v135, v135, v28\n"
    "v_sub_f32 v136, v136, v28\n"
    "v_sub_f32 v137, v137, v28\n"
    "v_sub_f32 v138, v138, v28\n"
    "v_sub_f32 v139, v139, v28\n"
    "v_sub_f32 v140, v140, v28\n"
    "v_sub_f32 v141, v141, v28\n"
    "v_sub_f32 v142, v142, v28\n"
    "v_sub_f32 v143, v143, v28\n"
    "v_exp_f32 v128, v128\n"
    "v_exp_f32 v129, v129\n"
    "v_exp_f32 v130, v130\n"
    "v_exp_f32 v131, v131\n"
    "v_exp_f32 v132, v132\n"
    "v_exp_f32 v133, v133\n"
    "v_exp_f32 v134, v134\n"
    "v_exp_f32 v135, v135\n"
    "v_exp_f32 v136, v136\n"
    "v_exp_f32 v137, v137\n"
    "v_exp_f32 v138, v138\n"
    "v_exp_f32 v139, v139\n"
    "v_exp_f32 v140, v140\n"
    "v_exp_f32 v141, v141\n"
    "v_exp_f32 v142, v142\n"
    "v_exp_f32 v143, v143\n"
    "v_sub_f32 v144, v144, v29\n"
    "v_sub_f32 v145, v145, v29\n"
    "v_sub_f32 v146, v146, v29\n"
    "v_sub_f32 v147, v147, v29\n"
    "v_sub_f32 v148, v148, v29\n"
    "v_sub_f32 v149, v149, v29\n"
    "v_sub_f32 v150, v150, v29\n"
    "v_sub_f32 v151, v151, v29\n"
    "v_sub_f32 v152, v152, v29\n"
    "v_sub_f32 v153, v153, v29\n"
    "v_sub_f32 v154, v154, v29\n"
    "v_sub_f32 v155, v155, v29\n"
    "v_sub_f32 v156, v156, v29\n"
    "v_sub_f32 v157, v157, v29\n"
    "v_sub_f32 v158, v158, v29\n"
    "v_sub_f32 v159, v159, v29\n"
    "v_exp_f32 v144, v144\n"
    "v_exp_f32 v145, v145\n"
    "v_exp_f32 v146, v146\n"
    "v_exp_f32 v147, v147\n"
    "v_exp_f32 v148, v148\n"
    "v_exp_f32 v149, v149\n"
    "v_exp_f32 v150, v150\n"
    "v_exp_f32 v151, v151\n"
    "v_exp_f32 v152, v152\n"
    "v_exp_f32 v153, v153\n"
    "v_exp_f32 v154, v154\n"
    "v_exp_f32 v155, v155\n"
    "v_exp_f32 v156, v156\n"
    "v_exp_f32 v157, v157\n"
    "v_exp_f32 v158, v158\n"
    "v_exp_f32 v159, v159\n"
    "v_sub_f32 v160, v160, v30\n"
    "v_sub_f32 v161, v161, v30\n"
    "v_sub_f32 v162, v162, v30\n"
    "v_sub_f32 v163, v163, v30\n"
    "v_sub_f32 v164, v164, v30\n"
    "v_sub_f32 v165, v165, v30\n"
    "v_sub_f32 v166, v166, v30\n"
    "v_sub_f32 v167, v167, v30\n"
    "v_sub_f32 v168, v168, v30\n"
    "v_sub_f32 v169, v169, v30\n"
    "v_sub_f32 v170, v170, v30\n"
    "v_sub_f32 v171, v171, v30\n"
    "v_sub_f32 v172, v172, v30\n"
    "v_sub_f32 v173, v173, v30\n"
    "v_sub_f32 v174, v174, v30\n"
    "v_sub_f32 v175, v175, v30\n"
    "v_exp_f32 v160, v160\n"
    "v_exp_f32 v161, v161\n"
    "v_exp_f32 v162, v162\n"
    "v_exp_f32 v163, v163\n"
    "v_exp_f32 v164, v164\n"
    "v_exp_f32 v165, v165\n"
    "v_exp_f32 v166, v166\n"
    "v_exp_f32 v167, v167\n"
    "v_exp_f32 v168, v168\n"
    "v_exp_f32 v169, v169\n"
    "v_exp_f32 v170, v170\n"
    "v_exp_f32 v171, v171\n"
    "v_exp_f32 v172, v172\n"
    "v_exp_f32 v173, v173\n"
    "v_exp_f32 v174, v174\n"
    "v_exp_f32 v175, v175\n"
    "v_sub_f32 v176, v176, v31\n"
    "v_sub_f32 v177, v177, v31\n"
    "v_sub_f32 v178, v178, v31\n"
    "v_sub_f32 v179, v179, v31\n"
    "v_sub_f32 v180, v180, v31\n"
    "v_sub_f32 v181, v181, v31\n"
    "v_sub_f32 v182, v182, v31\n"
    "v_sub_f32 v183, v183, v31\n"
    "v_sub_f32 v184, v184, v31\n"
    "v_sub_f32 v185, v185, v31\n"
    "v_sub_f32 v186, v186, v31\n"
    "v_sub_f32 v187, v187, v31\n"
    "v_sub_f32 v188, v188, v31\n"
    "v_sub_f32 v189, v189, v31\n"
    "v_sub_f32 v190, v190, v31\n"
    "v_sub_f32 v191, v191, v31\n"
    "v_exp_f32 v176, v176\n"
    "v_exp_f32 v177, v177\n"
    "v_exp_f32 v178, v178\n"
    "v_exp_f32 v179, v179\n"
    "v_exp_f32 v180, v180\n"
    "v_exp_f32 v181, v181\n"
    "v_exp_f32 v182, v182\n"
    "v_exp_f32 v183, v183\n"
    "v_exp_f32 v184, v184\n"
    "v_exp_f32 v185, v185\n"
    "v_exp_f32 v186, v186\n"
    "v_exp_f32 v187, v187\n"
    "v_exp_f32 v188, v188\n"
    "v_exp_f32 v189, v189\n"
    "v_exp_f32 v190, v190\n"
    "v_exp_f32 v191, v191\n"
    "v_smfmac_f32_32x32x32_bf16 v[192:207], v[72:75], v[128:135], v16\n"
    "v_smfmac_f32_32x32x32_bf16 v[192:207], v[76:79], v[136:143], v16\n"
    "v_smfmac_f32_32x32x32_bf16 v[208:223], v[72:75], v[144:151], v16\n"
    "v_smfmac_f32_32x32x32_bf16 v[208:223], v[76:79], v[152:159], v16\n"
    "v_smfmac_f32_32x32x32_bf16 v[224:239], v[72:75], v[160:167], v16\n"
    "v_smfmac_f32_32x32x32_bf16 v[224:239], v[76:79], v[168:175], v16\n"
    "v_smfmac_f32_32x32x32_bf16 v[240:255], v[72:75], v[176:183], v16\n"
    "v_smfmac_f32_32x32x32_bf16 v[240:255], v[76:79], v[184:191], v16\n"
    "s_add_u32 s52, s52, 61440\n"
    "s_cmp_lt_u32 s52, 983040\n"
    "s_cbranch_scc1 .Lfb_loop2\n"
    "s_nop 2\n"
    "ds_write_b128 v8, v[192:195] offset:0\n"
    "ds_write_b128 v8, v[196:199] offset:1024\n"
    "ds_write_b128 v8, v[200:203] offset:2048\n"
    "ds_write_b128 v8, v[204:207] offset:3072\n"
    "ds_write_b128 v8, v[208:211] offset:4096\n"
    "ds_write_b128 v8, v[212:215] offset:5120\n"
    "ds_write_b128 v8, v[216:219] offset:6144\n"
    "ds_write_b128 v8, v[220:223] offset:7168\n"
    "ds_write_b128 v8, v[224:227] offset:8192\n"
    "ds_write_b128 v8, v[228:231] offset:9216\n"
    "ds_write_b128 v8, v[232:235] offset:10240\n"
    "ds_write_b128 v8, v[236:239] offset:11264\n"
    "s_branch .Lcomb\n"
    : "+{s[0:1]}"(ka), "+{v0}"(tid)
    : "{s2}"(__builtin_amdgcn_workgroup_id_x())
    : "v1", "v2", "v3", "v4", "v5", "v6", "v7", "v8", "v9", "v10", "v11", "v12", "v13", "v14", "v15", "v16", "v17", "v18", "v19", "v20", "v21", "v22", "v23", "v24", "v25", "v26", "v27", "v28", "v29", "v30", "v31", "v32", "v33", "v34", "v35", "v36", "v37", "v38", "v39", "v40", "v41", "v42", "v43", "v44", "v45", "v46", "v47", "v48", "v49", "v50", "v51", "v52", "v53", "v54", "v55", "v56", "v57", "v58", "v59", "v60", "v61", "v62", "v63", "v64", "v65", "v66", "v67", "v68", "v69", "v70", "v71", "v72", "v73", "v74", "v75", "v76", "v77", "v78", "v79", "v80", "v81", "v82", "v83", "v84", "v85", "v86", "v87", "v88", "v89", "v90", "v91", "v92", "v93", "v94", "v95", "v96", "v97", "v98", "v99", "v100", "v101", "v102", "v103", "v104", "v105", "v106", "v107", "v108", "v109", "v110", "v111", "v112", "v113", "v114", "v115", "v116", "v117", "v118", "v119", "v120", "v121", "v122", "v123", "v124", "v125", "v126", "v127", "v128", "v129", "v130", "v131", "v132", "v133", "v134", "v135", "v136", "v137", "v138", "v139", "v140", "v141", "v142", "v143", "v144", "v145", "v146", "v147", "v148", "v149", "v150", "v151", "v152", "v153", "v154", "v155", "v156", "v157", "v158", "v159", "v160", "v161", "v162", "v163", "v164", "v165", "v166", "v167", "v168", "v169", "v170", "v171", "v172", "v173", "v174", "v175", "v176", "v177", "v178", "v179", "v180", "v181", "v182", "v183", "v184", "v185", "v186", "v187", "v188", "v189", "v190", "v191", "v192", "v193", "v194", "v195", "v196", "v197", "v198", "v199", "v200", "v201", "v202", "v203", "v204", "v205", "v206", "v207", "v208", "v209", "v210", "v211", "v212", "v213", "v214", "v215", "v216", "v217", "v218", "v219", "v220", "v221", "v222", "v223", "v224", "v225", "v226", "v227", "v228", "v229", "v230", "v231", "v232", "v233", "v234", "v235", "v236", "v237", "v238", "v239", "v240", "v241", "v242", "v243", "v244", "v245", "v246", "v247", "v248", "v249", "v250", "v251", "v252", "v253", "v254", "v255", "s3", "s4", "s5", "s6", "s7", "s8", "s9", "s10", "s11", "s12", "s13", "s14", "s15", "s16", "s17", "s18", "s19", "s20", "s21", "s22", "s23", "s24", "s25", "s26", "s27", "s28", "s29", "s30", "s31", "s32", "s33", "s34", "s35", "s36", "s37", "s38", "s39", "s40", "s41", "s42", "s43", "s44", "s45", "s46", "s47", "s48", "s49", "s50", "s51", "s52", "s53", "s54", "s55", "s56", "s57", "s58", "s59", "s60", "s61", "s62", "s63", "s64", "s65", "s66", "s67", "s68", "s69", "s70", "s71", "s72", "s73", "s74", "s75", "s76", "s77", "s78", "s79", "vcc", "scc", "memory");
}

extern "C" void kernel_launch(void* const* d_in, const int* in_sizes, int n_in, void* d_out, int out_size,
                              void* d_ws, size_t ws_size, hipStream_t stream) {
  const unsigned R = 0x00020000u;
  gated_mix_fused<<<256, 512, 0, stream>>>(
      d_in[0], 2u * in_sizes[0], R, d_in[1], 2u * in_sizes[1], R, d_in[7], 2u * in_sizes[7], R, d_ws, 24117248u, R,
      d_in[4], 2u * in_sizes[4], R, d_in[5], 2u * in_sizes[5], R, d_in[6], 2u * in_sizes[6], R, d_in[2], 2u * in_sizes[2], R,
      d_out, 2u * out_size, R);
}
```
